```python
import torch

def _softmax(x, axis=-1):
    return torch.softmax(x, dim=axis)

def _gelu(x):
    return torch.nn.functional.gelu(x, approximate='tanh')

def _silu(x):
    return torch.nn.functional.silu(x)

def _sigmoid(x):
    return torch.sigmoid(x)

def _layernorm(x, axis=-1, eps=1e-05):
    m = x.mean(dim=axis, keepdim=True)
    v = x.var(dim=axis, keepdim=True, unbiased=False)
    return (x - m) / torch.sqrt(v + eps)

def _scatter_add(tgt, idx, src, axis=0):
    return tgt.clone().index_add_(axis, idx, src)

def _segment_sum(src, seg, n):
    out = torch.zeros((n,) + src.shape[1:], dtype=src.dtype)
    return out.index_add_(0, seg, src)

def _pad(x, pw):
    flat = tuple((v for pair in reversed(pw) for v in pair))
    return torch.nn.functional.pad(x, flat)
kernel_name = 'adaLN_cond__spectral_gfnet__glu_gated_geglu__prompt'

def setup_inputs():

    _w = lambda a: (lambda t: t.to(torch.bfloat16) if t.is_floating_point() else t)(torch.as_tensor(a))

    def _A_setup():
        B, S, D = (1, 2048, 640)
        Df = D // 2 + 1
        return {'x': _w((lambda *s: torch.randn(*s, dtype=torch.bfloat16) * 0.05)(B, S, D)), 'w_r': _w((lambda *s: torch.randn(*s, dtype=torch.bfloat16) * 3.2)(S, Df) * 0.02), 'w_i': _w((lambda *s: torch.randn(*s, dtype=torch.bfloat16) * 3.2)(S, Df) * 0.02)}
    _A_in = _A_setup()

    def _B_setup():
        B, S, Dm, Df = (1, 2048, 640, 1536)
        return {'x': _w((lambda *s: torch.randn(*s, dtype=torch.bfloat16) * 0.05)(B, S, Dm)), 'w_gate': _w((lambda *s: torch.randn(*s, dtype=torch.bfloat16) * 0.4)(Dm, Df) * Dm ** (-0.5)), 'w_up': _w((lambda *s: torch.randn(*s, dtype=torch.bfloat16) * 0.4)(Dm, Df) * Dm ** (-0.5)), 'w_down': _w((lambda *s: torch.randn(*s, dtype=torch.bfloat16) * 0.4)(Df, Dm) * Df ** (-0.5))}
    _B_in = _B_setup()
    return {'A_x': _A_in['x'], 'A_w_r': _A_in['w_r'], 'A_w_i': _A_in['w_i'], 'B_x': _B_in['x'], 'B_w_gate': _B_in['w_gate'], 'B_w_up': _B_in['w_up'], 'B_w_down': _B_in['w_down'], 'c': _w((lambda *s: torch.randn(*s, dtype=torch.bfloat16) * 0.1)(1, 640) * 8.0), 'w_mod': _w((lambda *s: torch.randn(*s, dtype=torch.bfloat16) * 0.1)(640, 6 * 640) * 640 ** (-0.5) * 8.0)}

def reference(A_x, A_w_r, A_w_i, B_x, B_w_gate, B_w_up, B_w_down, c, w_mod):

    def _A_ref(x, w_r, w_i):
        B, S, D = x.shape
        X = (lambda x, axes=(-2, -1): torch.fft.rfft2(x.float(), dim=axes))(x, axes=(-2, -1))
        W = w_r + 1j * w_i
        X = X * W
        y = (lambda x, s, axes=(-2, -1): torch.fft.irfft2(x, s=s, dim=axes).to(torch.bfloat16))(X, (S, D), axes=(-2, -1))
        return y

    def _B_ref(x, w_gate, w_up, w_down):
        g = torch.einsum('bsd,df->bsf', x, w_gate)
        u = torch.einsum('bsd,df->bsf', x, w_up)
        h = _gelu(g) * u
        return torch.einsum('bsf,fd->bsd', h, w_down)
    x = A_x
    mod = (_silu(c) @ w_mod).reshape(c.shape[0], 6, 1, -1)
    sa, ca, ga, sb, cb, gb = (mod[:, 0].clone(), mod[:, 1].clone(), mod[:, 2].clone(), mod[:, 3].clone(), mod[:, 4].clone(), mod[:, 5].clone())
    h = _layernorm(x, axis=-1) * (1 + ca) + sa
    x = x + ga * _A_ref(h, A_w_r, A_w_i)
    h = _layernorm(x, axis=-1) * (1 + cb) + sb
    x = x + gb * _B_ref(h, B_w_gate, B_w_up, B_w_down)
    return x
```

```cpp
#include <hip/hip_runtime.h>

typedef unsigned short u16;
typedef unsigned int u32;
typedef __bf16 bf16x8 __attribute__((ext_vector_type(8)));
typedef __bf16 bf16x2 __attribute__((ext_vector_type(2)));
typedef float f32x2 __attribute__((ext_vector_type(2)));
typedef float f32x4 __attribute__((ext_vector_type(4)));
typedef float f32x16 __attribute__((ext_vector_type(16)));
typedef u32 u32x2 __attribute__((ext_vector_type(2)));
typedef u32 u32x4 __attribute__((ext_vector_type(4)));
typedef short s16x4 __attribute__((ext_vector_type(4)));
#define LAS __attribute__((address_space(3)))
#define DI __device__ __forceinline__

constexpr int S_ = 2048, D_ = 640, FF_ = 1536;
constexpr size_t MOD_OFF = 0;
constexpr size_t CS_OFF = 16384;
constexpr size_t BS_OFF = 32768;
constexpr size_t STAT_OFF = 65536;
constexpr size_t U_OFF = 262144;
constexpr size_t UP_OFF = U_OFF + 2621440;
constexpr size_t X1_OFF = UP_OFF + 2662400;
constexpr size_t WP_OFF = X1_OFF + 2621440;
constexpr size_t WD_OFF = WP_OFF + 3932160;
constexpr size_t HH_OFF = WD_OFF + 1966080;

DI u32 pk2(float a, float b) {
  f32x2 v = {a, b};
  return __builtin_bit_cast(u32, __builtin_convertvector(v, bf16x2));
}
DI float rbf(float a) { return __builtin_bit_cast(float, pk2(a, 0.f) << 16); }
DI float lo2f(u32 v) { return __builtin_bit_cast(float, v << 16); }
DI float hi2f(u32 v) { return __builtin_bit_cast(float, v & 0xffff0000u); }
DI f32x4 mfma16(u32x4 a, u32x4 b, f32x4 c) {
  return __builtin_amdgcn_mfma_f32_16x16x32_bf16(__builtin_bit_cast(bf16x8, a), __builtin_bit_cast(bf16x8, b), c, 0, 0, 0);
}
DI f32x16 mfma32(u32x4 a, u32x4 b, f32x16 c) {
  return __builtin_amdgcn_mfma_f32_32x32x16_bf16(__builtin_bit_cast(bf16x8, a), __builtin_bit_cast(bf16x8, b), c, 0, 0, 0);
}
DI u32x2 trread(const char* p) {
  s16x4 r = __builtin_amdgcn_ds_read_tr16_b64_v4i16((LAS s16x4*)(p));
  return __builtin_bit_cast(u32x2, r);
}
DI float cosr(float rev) { return __builtin_amdgcn_cosf(__builtin_amdgcn_fractf(rev)); }
DI float sinr(float rev) { return __builtin_amdgcn_sinf(__builtin_amdgcn_fractf(rev)); }
DI float dot2(u32 a, u32 b, float c) {
  return __builtin_amdgcn_fdot2_f32_bf16(__builtin_bit_cast(bf16x2, a), __builtin_bit_cast(bf16x2, b), c, false);
}
template <int CTRL> DI float dppf(float v) {
  return __builtin_bit_cast(float, __builtin_amdgcn_update_dpp(0, __builtin_bit_cast(int, v), CTRL, 0xf, 0xf, true));
}

__global__ __launch_bounds__(512) void k1_ln_dft128(const u16* __restrict__ x, const u16* __restrict__ cvec,
                                                     const u16* __restrict__ wmod, char* __restrict__ ws) {
  __shared__ __attribute__((aligned(16))) char sm[16384];
  const int tid = threadIdx.x, xcd = blockIdx.x & 7, idx = blockIdx.x >> 3;
  if (idx < 20) {
    const int s2 = 2 * xcd + idx / 10, cg = idx % 10;
    const int w = tid >> 6, l = tid & 63;
    const int j8 = tid & 7, rr = tid >> 3;
    u32x4 v[2][10];
#pragma unroll
    for (int ps = 0; ps < 2; ++ps) {
      const u16* row = x + (size_t)(16 * (ps * 64 + rr) + s2) * D_;
#pragma unroll
      for (int i = 0; i < 10; ++i) {
        int c = cg + i;
        c = c >= 10 ? c - 10 : c;
        v[ps][i] = *(const u32x4*)(row + c * 64 + j8 * 8);
      }
    }
    __builtin_amdgcn_sched_barrier(0);
    const int Mt = w & 3, nt = w >> 2, h = l >> 5;
    u32x4 ac[8];
    {
      const int row = l & 31, j = 16 * Mt + (row >> 1), part = row & 1;
      const int jj = (part && j == 0) ? 64 : j;
      const float ph = (part && j != 0) ? 0.25f : 0.f;
      const float step = (float)jj * (1.f / 128.f);
#pragma unroll
      for (int kap = 0; kap < 8; ++kap) {
        float cv[8];
#pragma unroll
        for (int e = 0; e < 8; ++e) cv[e] = cosr(fmaf(step, (float)(16 * kap + 8 * h + e), ph));
#pragma unroll
        for (int e = 0; e < 4; ++e) ac[kap][e] = pk2(cv[2 * e], cv[2 * e + 1]);
      }
    }
#pragma unroll
    for (int ps = 0; ps < 2; ++ps) {
      float s = 0.f, q = 0.f;
#pragma unroll
      for (int i = 0; i < 10; ++i)
#pragma unroll
        for (int e = 0; e < 4; ++e) {
          s = dot2(v[ps][i][e], 0x3f803f80u, s);
          q = dot2(v[ps][i][e], v[ps][i][e], q);
        }
      s += dppf<0xB1>(s); q += dppf<0xB1>(q);
      s += dppf<0x4E>(s); q += dppf<0x4E>(q);
      s += dppf<0x141>(s); q += dppf<0x141>(q);
      const float m = s * (1.f / 640.f);
      const float var = fmaxf(q * (1.f / 640.f) - m * m, 0.f);
      const float mb = rbf(m);
      const float sd = rbf(sqrtf(rbf(rbf(var) + 1e-5f)));
      const float r = 1.f / sd, mr = -mb * r;
      u32x4 o;
#pragma unroll
      for (int e = 0; e < 4; ++e) o[e] = pk2(fmaf(lo2f(v[ps][0][e]), r, mr), fmaf(hi2f(v[ps][0][e]), r, mr));
      const int s1 = ps * 64 + rr;
      *(u32x4*)(sm + s1 * 128 + ((j8 ^ (((s1 >> 1) & 1) << 2)) << 4)) = o;
    }
    __syncthreads();
    f32x16 acc;
#pragma unroll
    for (int i = 0; i < 16; ++i) acc[i] = 0.f;
    {
      const int blk = (l >> 4) & 1, q = (l & 15) >> 2, p = l & 3;
      const int colb = (64 * nt + 32 * blk + 8 * p) ^ (((q >> 1) & 1) << 6);
      const char* base = sm + (8 * h + q) * 128 + colb;
#pragma unroll
      for (int kap = 0; kap < 8; ++kap) {
        u32x2 b0 = trread(base + kap * 2048), b1 = trread(base + kap * 2048 + 512);
        u32x4 b = {b0[0], b0[1], b1[0], b1[1]};
        acc = mfma32(ac[kap], b, acc);
      }
    }
    u32* U = (u32*)(ws + U_OFF);
#pragma unroll
    for (int i = 0; i < 16; i += 2) {
      const int j = 16 * Mt + ((i & 3) >> 1) + 4 * (i >> 2) + 2 * h;
      U[(size_t)(j * 16 + s2) * D_ + 64 * cg + 32 * nt + (l & 31)] = pk2(acc[i], acc[i + 1]);
    }
  } else {
    const int id = xcd * 12 + (idx - 20);
    float* red = (float*)sm;
    if (tid < 320) {
      const int rs = tid / 5, j5 = tid % 5;
      u32x4 wv[10];
      u16 cv[10];
#pragma unroll
      for (int i = 0; i < 10; ++i) {
        wv[i] = *(const u32x4*)(wmod + (size_t)(rs + 64 * i) * 3840 + 40 * id + 8 * j5);
        cv[i] = cvec[rs + 64 * i];
      }
      float a[8];
#pragma unroll
      for (int e = 0; e < 8; ++e) a[e] = 0.f;
#pragma unroll
      for (int i = 0; i < 10; ++i) {
        const float c = lo2f(cv[i]);
        const float sv = rbf(c / (1.f + __expf(-c)));
#pragma unroll
        for (int e = 0; e < 4; ++e) {
          a[2 * e] = fmaf(sv, lo2f(wv[i][e]), a[2 * e]);
          a[2 * e + 1] = fmaf(sv, hi2f(wv[i][e]), a[2 * e + 1]);
        }
      }
#pragma unroll
      for (int e = 0; e < 8; ++e) red[rs * 40 + j5 * 8 + e] = a[e];
    }
    __syncthreads();
    if (tid < 320) {
      const int col = tid % 40, part = tid / 40;
      float s = 0.f;
#pragma unroll
      for (int i = 0; i < 8; ++i) s += red[(part * 8 + i) * 40 + col];
      red[2560 + part * 40 + col] = s;
    }
    __syncthreads();
    if (tid < 40) {
      float s = 0.f;
#pragma unroll
      for (int i = 0; i < 8; ++i) s += red[2560 + i * 40 + tid];
      ((float*)(ws + MOD_OFF))[40 * id + tid] = rbf(s);
    }
  }
}

constexpr int VP = 1320;
constexpr int CT_OFF = 21504;
enum { SL_D1 = 0, SL_MM = 2, SL_ME = 4, SL_EM = 8, SL_EE = 12, SL_D1I = 14, SL_CI = 16, SL_TFM = 18, SL_TFE = 20, SL_TIM = 22, SL_TIE = 24 };

DI void cmulc(float& re, float& im, float c, float s) {
  const float a = re * c + im * s, b = im * c - re * s;
  re = a; im = b;
}
DI u32x4 packf(const f32x4& re, const f32x4& im) {
  u32x4 r = {pk2(re[0], re[1]), pk2(re[2], re[3]), pk2(im[0], im[1]), pk2(im[2], im[3])};
  return r;
}

__global__ __launch_bounds__(512) void k2_spec(const u16* __restrict__ wr_, const u16* __restrict__ wi_,
                                               const u16* __restrict__ wg, const u16* __restrict__ wu,
                                               char* __restrict__ ws) {
  __shared__ __attribute__((aligned(16))) char sm[CT_OFF + 26 * 1024];
  const int tid = threadIdx.x, xcd = blockIdx.x & 7, idx = blockIdx.x >> 3;
  const float* modf = (const float*)(ws + MOD_OFF);
  if (idx < 18) {
    const int k1 = xcd + 8 * (idx >> 1), p = idx & 1;
    if (k1 > 64) return;
    const int w = __builtin_amdgcn_readfirstlane(tid >> 6), l = tid & 63, ln = l & 15, g = l >> 4;
    const u32* U = (const u32*)(ws + U_OFF) + (size_t)(k1 & 63) * 16 * D_;
    u32x4 ua[3][2];
    f32x4 cav[3][2];
#pragma unroll
    for (int t = 0; t < 3; ++t) {
      const int u = (t < 2 || w < 4) ? w + 8 * t : w;
#pragma unroll
      for (int hf = 0; hf < 2; ++hf) {
#pragma unroll
        for (int i = 0; i < 4; ++i) ua[t][hf][i] = U[(4 * g + i) * D_ + 16 * u + 320 * hf + ln];
        cav[t][hf] = *(const f32x4*)(modf + 640 + 16 * u + 320 * hf + 4 * g);
      }
    }
    float fr[3][4], fi[3][4];
#pragma unroll
    for (int t = 0; t < 3; ++t) {
      const int rho = 2 * w + (t < 2 ? t : (g & 1));
      const int ks = k1 + 128 * rho, ksm = (2048 - ks) & 2047;
#pragma unroll
      for (int r = 0; r < 4; ++r) {
        const int kb = t < 2 ? 4 * g + r : 16 + r;
        const int kd = 2 * ln + p + 32 * kb;
        const bool dir = kd <= 320;
        const int off = dir ? ks * 321 + kd : ksm * 321 + (640 - kd);
        fr[t][r] = lo2f(wr_[off]);
        const float im = lo2f(wi_[off]);
        fi[t][r] = dir ? im : -im;
      }
    }
    if (p == 0 && ln == 0 && (g == 0 || g == 2)) {
#pragma unroll
      for (int t = 0; t < 2; ++t) {
        const int ks = k1 + 128 * (2 * w + t), ksm = (2048 - ks) & 2047;
        const int off = ksm * 321 + (g == 0 ? 0 : 320);
        const float a = lo2f(wr_[off]), b = lo2f(wi_[off]);
        if (g == 0) { fr[t][0] = 0.5f * (fr[t][0] + a); fi[t][0] = 0.5f * (fi[t][0] - b); }
        else        { fr[t][2] = 0.5f * (fr[t][2] + a); fi[t][2] = 0.5f * (fi[t][2] - b); }
      }
    }
    char* ct = sm + CT_OFF + l * 16;
#pragma unroll
    for (int rep = 0; rep < 2; ++rep) {
      const int set = rep == 0 ? w : w + 4;
      if (rep == 1 && (w < 4 || w == 7)) break;
      float base, step;
      const float fln = (float)ln, fg4 = (float)(4 * g), fp = (float)p, fe = (float)(16 + (ln & 3));
      switch (set) {
        case 0: step = (2.f * fln + fp) * (1.f / 32.f); base = fg4 * step; break;
        case 1: step = fln * (1.f / 20.f); base = fg4 * step; break;
        case 2: step = fln * (1.f / 20.f); base = 16.f * step; break;
        case 3: step = fe * (1.f / 20.f); base = fg4 * step; break;
        case 4: step = fe * (1.f / 20.f); base = 16.f * step; break;
        case 5: step = fln * (1.f / 16.f); base = fln * (2.f * fg4 + fp) * (1.f / 32.f); break;
        case 6: step = fln * (1.f / 16.f); base = fln * (float)k1 * (1.f / 2048.f) + fg4 * step; break;
        case 7: step = (2.f * fln + fp) * (1.f / 640.f); base = fg4 * step; break;
        case 8: step = (2.f * fln + fp) * (1.f / 640.f); base = 16.f * step; break;
        case 9: step = fln * (1.f / 320.f); base = fln * (2.f * fg4 + fp) * (1.f / 640.f); break;
        default: step = fe * (1.f / 320.f); base = fe * (2.f * fg4 + fp) * (1.f / 640.f); break;
      }
      f32x4 cs, sn;
#pragma unroll
      for (int e = 0; e < 4; ++e) {
        const float a = __builtin_amdgcn_fractf(fmaf(step, (float)e, base));
        cs[e] = __builtin_amdgcn_cosf(a);
        sn[e] = __builtin_amdgcn_sinf(a);
      }
      if (set >= 7) {
        const float sc = set >= 9 ? (1.f / (2048.f * 640.f)) : 1.f;
        const int sl = SL_TFM + 2 * (set - 7);
        *(f32x4*)(ct + sl * 1024) = cs * sc;
        *(f32x4*)(ct + (sl + 1) * 1024) = sn * sc;
      } else {
        const u32 c01 = pk2(cs[0], cs[1]), c23 = pk2(cs[2], cs[3]), s01 = pk2(sn[0], sn[1]), s23 = pk2(sn[2], sn[3]);
        const u32x4 fre = {c01, c23, s01, s23}, fim = {s01 ^ 0x80008000u, s23 ^ 0x80008000u, c01, c23};
        const u32x4 z = {0u, 0u, 0u, 0u};
        if (set == 2 || set == 3) {
          const int sel = set == 2 ? g : (ln >> 2);
          const int sl = set == 2 ? SL_ME : SL_EM;
          *(u32x4*)(ct + (sl + 0) * 1024) = sel == 0 ? fre : z;
          *(u32x4*)(ct + (sl + 1) * 1024) = sel == 0 ? fim : z;
          *(u32x4*)(ct + (sl + 2) * 1024) = sel == 1 ? fre : z;
          *(u32x4*)(ct + (sl + 3) * 1024) = sel == 1 ? fim : z;
        } else {
          const bool keep = set != 4 || ((ln >> 2) == g && g < 2);
          const int sl = set == 0 ? SL_D1 : set == 1 ? SL_MM : set == 4 ? SL_EE : set == 5 ? SL_D1I : SL_CI;
          *(u32x4*)(ct + sl * 1024) = keep ? fre : z;
          *(u32x4*)(ct + (sl + 1) * 1024) = keep ? fim : z;
        }
      }
    }
    {
      u32x4 cre, cim;
      const float step = (float)(k1 + 128 * ln) * (1.f / 2048.f);
#pragma unroll
      for (int i = 0; i < 4; ++i) {
        const float a = __builtin_amdgcn_fractf(step * (float)(4 * g + i));
        const float c = __builtin_amdgcn_cosf(a), s = __builtin_amdgcn_sinf(a);
        if (k1 == 0) { cre[i] = pk2(c, 0.f); cim[i] = pk2(-s, 0.f); }
        else if (k1 == 64) { cre[i] = pk2(0.f, c); cim[i] = pk2(0.f, -s); }
        else { cre[i] = pk2(c, s); cim[i] = pk2(-s, c); }
      }
      const float sg = p ? -1.f : 1.f;
      const f32x4 z4 = {0.f, 0.f, 0.f, 0.f};
#pragma unroll
      for (int t = 0; t < 3; ++t) {
        if (t == 2 && w >= 4) break;
        const int u = w + 8 * t;
        const f32x4 r1 = mfma16(ua[t][0], cre, z4), i1 = mfma16(ua[t][0], cim, z4);
        const f32x4 r2 = mfma16(ua[t][1], cre, z4), i2 = mfma16(ua[t][1], cim, z4);
        f32x4 vr, vi;
#pragma unroll
        for (int r = 0; r < 4; ++r) {
          const float c1 = rbf(1.f + cav[t][0][r]), c2 = sg * rbf(1.f + cav[t][1][r]);
          vr[r] = c1 * r1[r] + c2 * r2[r];
          vi[r] = c1 * i1[r] + c2 * i2[r];
        }
        if (k1 == 0 && ln == 0) {
          const f32x4 sa1 = *(const f32x4*)(modf + 16 * u + 4 * g), sa2 = *(const f32x4*)(modf + 16 * u + 320 + 4 * g);
#pragma unroll
          for (int r = 0; r < 4; ++r) vr[r] += 2048.f * (sa1[r] + sg * sa2[r]);
        }
        char* dst = sm + ln * VP + (16 * u + 4 * g) * 2;
        *(u32x2*)(dst) = u32x2{pk2(vr[0], vr[1]), pk2(vr[2], vr[3])};
        *(u32x2*)(dst + 640) = u32x2{pk2(vi[0], vi[1]), pk2(vi[2], vi[3])};
      }
    }
    __syncthreads();
    {
      const int q = ln >> 2, pp = l & 3;
      const f32x4 z4 = {0.f, 0.f, 0.f, 0.f};
#define CT(sl) (*(const u32x4*)(ct + (sl) * 1024))
#define CTF(sl) (*(const f32x4*)(ct + (sl) * 1024))
      u32x4 y[3];
      {
        const u32x4 bre = CT(SL_D1), bim = CT(SL_D1 + 1);
#pragma unroll
        for (int t = 0; t < 3; ++t) {
          const char* src = t < 2 ? sm + (2 * w + t) * VP + ((4 * g + q) * 20 + 4 * pp) * 2
                                  : sm + (2 * w + (pp & 1)) * VP + ((4 * g + q) * 20 + 16) * 2;
          const u32x2 a0 = trread(src), a1 = trread(src + 640);
          const u32x4 a = {a0[0], a0[1], a1[0], a1[1]};
          f32x4 re = mfma16(a, bre, z4), im = mfma16(a, bim, z4);
          const f32x4 tc = CTF(t < 2 ? SL_TFM : SL_TFE), ts = CTF((t < 2 ? SL_TFM : SL_TFE) + 1);
#pragma unroll
          for (int r = 0; r < 4; ++r) { float a_ = re[r], b_ = im[r]; cmulc(a_, b_, tc[r], ts[r]); re[r] = a_; im[r] = b_; }
          y[t] = packf(re, im);
        }
      }
      const u32x4 mm0 = CT(SL_MM), mm1 = CT(SL_MM + 1), ee0 = CT(SL_EE), ee1 = CT(SL_EE + 1);
      u32x4 me[4], em[4];
#pragma unroll
      for (int i = 0; i < 4; ++i) { me[i] = CT(SL_ME + i); em[i] = CT(SL_EM + i); }
      u32x4 yf[3];
      {
        f32x4 re[3], im[3];
#pragma unroll
        for (int v = 0; v < 2; ++v) {
          re[v] = mfma16(me[2 * v], y[2], mfma16(mm0, y[v], z4));
          im[v] = mfma16(me[2 * v + 1], y[2], mfma16(mm1, y[v], z4));
        }
        re[2] = mfma16(ee0, y[2], mfma16(em[2], y[1], mfma16(em[0], y[0], z4)));
        im[2] = mfma16(ee1, y[2], mfma16(em[3], y[1], mfma16(em[1], y[0], z4)));
#pragma unroll
        for (int t = 0; t < 3; ++t) {
          f32x4 a, b;
#pragma unroll
          for (int r = 0; r < 4; ++r) {
            a[r] = re[t][r] * fr[t][r] - im[t][r] * fi[t][r];
            b[r] = -(re[t][r] * fi[t][r] + im[t][r] * fr[t][r]);
          }
          yf[t] = packf(a, b);
        }
      }
      {
        f32x4 re[3], im[3];
#pragma unroll
        for (int v = 0; v < 2; ++v) {
          re[v] = mfma16(yf[2], me[2 * v], mfma16(yf[v], mm0, z4));
          im[v] = mfma16(yf[2], me[2 * v + 1], mfma16(yf[v], mm1, z4));
        }
        re[2] = mfma16(yf[2], ee0, mfma16(yf[1], em[2], mfma16(yf[0], em[0], z4)));
        im[2] = mfma16(yf[2], ee1, mfma16(yf[1], em[3], mfma16(yf[0], em[1], z4)));
        const u32x4 bre = CT(SL_D1I), bim = CT(SL_D1I + 1);
#pragma unroll
        for (int t = 0; t < 3; ++t) {
          const f32x4 tc = CTF(t < 2 ? SL_TIM : SL_TIE), ts = CTF((t < 2 ? SL_TIM : SL_TIE) + 1);
#pragma unroll
          for (int r = 0; r < 4; ++r) { float a_ = re[t][r], b_ = im[t][r]; cmulc(a_, b_, tc[r], ts[r]); re[t][r] = a_; im[t][r] = b_; }
          const u32x4 a = packf(re[t], im[t]);
          const f32x4 vr = mfma16(a, bre, z4), vi = mfma16(a, bim, z4);
          char* dst = t < 2 ? sm + (2 * w + t) * VP + (20 * ln + 4 * g) * 2 : sm + (2 * w + (g & 1)) * VP + (20 * ln + 16) * 2;
          if (t < 2 || g < 2) {
            *(u32x2*)(dst) = u32x2{pk2(vr[0], vr[1]), pk2(vr[2], vr[3])};
            *(u32x2*)(dst + 640) = u32x2{pk2(vi[0], vi[1]), pk2(vi[2], vi[3])};
          }
        }
      }
    }
    __syncthreads();
    {
      const int q = ln >> 2, pp = l & 3;
      const f32x4 z4 = {0.f, 0.f, 0.f, 0.f};
      const u32x4 bre = CT(SL_CI), bim = CT(SL_CI + 1);
      u32* UP = (u32*)(ws + UP_OFF) + (size_t)((k1 * 2 + p) * 16 + ln) * 320 + 4 * g;
#pragma unroll
      for (int t = 0; t < 3; ++t) {
        if (t == 2 && w >= 4) break;
        const int u = w + 8 * t;
        const char* src = sm + (4 * g + q) * VP + (16 * u + 4 * pp) * 2;
        const u32x2 a0 = trread(src), a1 = trread(src + 640);
        const u32x4 a = {a0[0], a0[1], a1[0], a1[1]};
        const f32x4 re = mfma16(a, bre, z4), im = mfma16(a, bim, z4);
        *(u32x4*)(UP + 16 * u) = u32x4{pk2(re[0], im[0]), pk2(re[1], im[1]), pk2(re[2], im[2]), pk2(re[3], im[3])};
      }
    }
#undef CT
#undef CTF
  } else {
    const int id = xcd * 12 + (idx - 18);
    float* red = (float*)sm;
    if (tid < 320) {
      const int ng = tid & 3, kg = tid >> 2, mat = ng & 1, hf = ng >> 1;
      const int kstep = kg >> 2, g = kg & 3;
      const u16* W = (mat ? wu : wg) + 16 * id + 8 * hf;
      u32x4 v[8];
      float f[8], sbv[8];
#pragma unroll
      for (int e = 0; e < 8; ++e) {
        const int k = 32 * kstep + 16 * (e >> 2) + 4 * g + (e & 3);
        v[e] = *(const u32x4*)(W + (size_t)k * FF_);
        f[e] = rbf(1.f + modf[2560 + k]);
        sbv[e] = modf[1920 + k];
      }
      const int ffl = (16 * id + 8 * hf) % 96, j = id / 6, wv = ffl / 24, tile = (ffl % 24) >> 3;
      u32x4* dst = (u32x4*)(ws + WP_OFF) + (size_t)((((j * 4 + wv) * 20 + kstep) * 3 + tile) * 64 + 16 * g + 2 * mat);
#pragma unroll
      for (int c = 0; c < 8; ++c) {
        float wv_[8];
#pragma unroll
        for (int e = 0; e < 8; ++e) wv_[e] = (c & 1) ? hi2f(v[e][c >> 1]) : lo2f(v[e][c >> 1]);
        u32x4 o;
        float cs = 0.f, bs = 0.f;
#pragma unroll
        for (int e = 0; e < 4; ++e) {
          o[e] = pk2(wv_[2 * e] * f[2 * e], wv_[2 * e + 1] * f[2 * e + 1]);
          cs = dot2(o[e], 0x3f803f80u, cs);
          bs = fmaf(sbv[2 * e], wv_[2 * e], bs);
          bs = fmaf(sbv[2 * e + 1], wv_[2 * e + 1], bs);
        }
        dst[4 * (c >> 1) + (c & 1)] = o;
        red[kg * 64 + ng * 8 + c] = cs;
        red[kg * 64 + 32 + ng * 8 + c] = bs;
      }
    }
    __syncthreads();
    if (tid < 320) {
      const int o = tid & 63, part = tid >> 6;
      float s = 0.f;
#pragma unroll
      for (int i = 0; i < 16; ++i) s += red[(part * 16 + i) * 64 + o];
      red[5120 + part * 64 + o] = s;
    }
    __syncthreads();
    if (tid < 64) {
      float s = 0.f;
#pragma unroll
      for (int i = 0; i < 5; ++i) s += red[5120 + i * 64 + tid];
      const int which = tid >> 5, ng = (tid >> 3) & 3, c = tid & 7, mat = ng & 1, hf = ng >> 1;
      const int ffl = (16 * id + 8 * hf) % 96, j = id / 6, wv = ffl / 24, tile = (ffl % 24) >> 3;
      float* dstv = (float*)(ws + (which ? BS_OFF : CS_OFF));
      dstv[((j * 4 + wv) * 3 + tile) * 16 + 4 * (c >> 1) + 2 * mat + (c & 1)] = s;
    }
  }
}

__global__ __launch_bounds__(512) void k3_idft128(const u16* __restrict__ x, const u16* __restrict__ wd, char* __restrict__ ws) {
  __shared__ __attribute__((aligned(16))) char sm[16384];
  const int tid = threadIdx.x, xcd = blockIdx.x & 7, idx = blockIdx.x >> 3;
  const float* modf = (const float*)(ws + MOD_OFF);
  if (idx < 20) {
    const int s2 = 4 * (xcd >> 1) + 2 * (idx / 10) + (xcd & 1), cg = idx % 10;
    const int w = __builtin_amdgcn_readfirstlane(tid >> 6), l = tid & 63, ln = l & 15, g = l >> 4;
    const u32* UP = (const u32*)(ws + UP_OFF);
    const int kap = w >> 1, mu = w & 1;
    u32x4 fa[2];
    u32 ny[2];
#pragma unroll
    for (int pp = 0; pp < 2; ++pp) {
#pragma unroll
      for (int i = 0; i < 4; ++i)
        fa[pp][i] = UP[(size_t)(((16 * kap + 4 * g + i) * 2 + pp) * 16 + s2) * 320 + 32 * cg + 16 * mu + ln];
      ny[pp] = UP[(size_t)((64 * 2 + pp) * 16 + s2) * 320 + 32 * cg + 16 * mu + ln];
    }
    const int s1 = 16 * w + ln;
    const u16* xrow = x + (size_t)(16 * s1 + s2) * D_ + 32 * cg + 4 * g;
    u32x2 xv[2][2];
    f32x4 gav[2][2];
#pragma unroll
    for (int hf = 0; hf < 2; ++hf)
#pragma unroll
      for (int m = 0; m < 2; ++m) {
        xv[hf][m] = *(const u32x2*)(xrow + 320 * hf + 16 * m);
        gav[hf][m] = *(const f32x4*)(modf + 1280 + 32 * cg + 320 * hf + 16 * m + 4 * g);
      }
    u32x4 bc[4];
    {
      const float step = (float)s1 * (1.f / 128.f);
#pragma unroll
      for (int kk = 0; kk < 4; ++kk)
#pragma unroll
        for (int i = 0; i < 4; ++i) {
          const float a = __builtin_amdgcn_fractf(step * (float)(16 * kk + 4 * g + i));
          float c = 2.f * __builtin_amdgcn_cosf(a), s = 2.f * __builtin_amdgcn_sinf(a);
          if (kk == 0 && i == 0 && g == 0) { c = 1.f; s = (s1 & 1) ? -1.f : 1.f; }
          bc[kk][i] = pk2(c, s);
        }
    }
#pragma unroll
    for (int pp = 0; pp < 2; ++pp) {
      if (kap == 0 && g == 0) fa[pp][0] = (fa[pp][0] & 0xffffu) | (ny[pp] << 16);
      *(u32x4*)(sm + (w * 2 + pp) * 1024 + l * 16) = fa[pp];
    }
    __syncthreads();
    f32x4 acc[2][2];
#pragma unroll
    for (int pp = 0; pp < 2; ++pp)
#pragma unroll
      for (int m = 0; m < 2; ++m) {
        f32x4 a = {0.f, 0.f, 0.f, 0.f};
#pragma unroll
        for (int kk = 0; kk < 4; ++kk) a = mfma16(*(const u32x4*)(sm + ((kk * 2 + m) * 2 + pp) * 1024 + l * 16), bc[kk], a);
        acc[pp][m] = a;
      }
    float ss = 0.f, qq = 0.f;
    u32x4* X1 = (u32x4*)(ws + X1_OFF);
#pragma unroll
    for (int hf = 0; hf < 2; ++hf) {
      u32x4 o;
#pragma unroll
      for (int m = 0; m < 2; ++m) {
        float x1[4];
#pragma unroll
        for (int r = 0; r < 4; ++r) {
          const float y = rbf(hf ? acc[0][m][r] - acc[1][m][r] : acc[0][m][r] + acc[1][m][r]);
          const float xx = (r & 1) ? hi2f(xv[hf][m][r >> 1]) : lo2f(xv[hf][m][r >> 1]);
          x1[r] = xx + rbf(gav[hf][m][r] * y);
        }
        o[2 * m] = pk2(x1[0], x1[1]);
        o[2 * m + 1] = pk2(x1[2], x1[3]);
      }
#pragma unroll
      for (int e = 0; e < 4; ++e) { ss = dot2(o[e], 0x3f803f80u, ss); qq = dot2(o[e], o[e], qq); }
      X1[(size_t)((s2 * 20 + cg + 10 * hf) * 8 + w) * 64 + l] = o;
    }
    ss += __shfl_xor(ss, 16); qq += __shfl_xor(qq, 16);
    ss += __shfl_xor(ss, 32); qq += __shfl_xor(qq, 32);
    if (g == 0) ((f32x2*)(ws + STAT_OFF))[(s2 * 128 + s1) * 10 + cg] = f32x2{ss, qq};
  } else {
    const int id = xcd * 12 + (idx - 20);
    if (tid < 160) {
      const int fg = 2 * id + tid / 80, cgp = tid % 80;
      u32x4 v[8];
#pragma unroll
      for (int e = 0; e < 8; ++e) v[e] = *(const u32x4*)(wd + (size_t)(8 * fg + e) * D_ + 8 * cgp);
      const int j = fg / 12, rem = fg % 12, gq = rem / 3, t = rem % 3;
      const int col = 8 * cgp, cgrp = col / 80, tile = (col % 80) >> 4, i0 = col & 15;
      u32x4* dst = (u32x4*)(ws + WD_OFF) + (size_t)(((cgrp * 48 + 3 * j + t) * 5 + tile) * 64 + 16 * gq + ((col % 80) & 15));
      (void)i0;
#pragma unroll
      for (int c = 0; c < 8; ++c) {
        u32x4 o;
#pragma unroll
        for (int e = 0; e < 4; ++e)
          o[e] = __builtin_amdgcn_perm(v[2 * e + 1][c >> 1], v[2 * e][c >> 1], (c & 1) ? 0x07060302u : 0x05040100u);
        dst[c] = o;
      }
    }
  }
}

constexpr int G1_PF = 5;
__global__ __launch_bounds__(256, 1) void k4_gemm1(char* __restrict__ ws) {
  __shared__ __attribute__((aligned(16))) char sm[65536 + 2048];
  const int tid = threadIdx.x, xcd = blockIdx.x & 7, idx = blockIdx.x >> 3;
  const int s2 = 4 * (xcd >> 1) + (idx & 3), j = 8 * (xcd & 1) + (idx >> 2);
  const int w = __builtin_amdgcn_readfirstlane(tid >> 6), l = tid & 63, ln = l & 15, g = l >> 4;
  const u32x4* Wp = (const u32x4*)(ws + WP_OFF) + (size_t)(j * 4 + w) * 60 * 64 + l;
  const u32x4* Xp = (const u32x4*)(ws + X1_OFF) + (size_t)s2 * 160 * 64 + (2 * w) * 64 + l;
  f32x4 st[5];
  if (tid < 128) {
    const f32x4* sp = (const f32x4*)(ws + STAT_OFF) + (s2 * 128 + tid) * 5;
#pragma unroll
    for (int i = 0; i < 5; ++i) st[i] = sp[i];
  }
  u32x4 wf[20][3], xf[20][2];
#pragma unroll
  for (int k = 0; k < G1_PF; ++k) {
    xf[k][0] = Xp[k * 512]; xf[k][1] = Xp[k * 512 + 64];
#pragma unroll
    for (int t = 0; t < 3; ++t) wf[k][t] = Wp[(k * 3 + t) * 64];
  }
  f32x4 csv[3], bsv[3];
#pragma unroll
  for (int t = 0; t < 3; ++t) {
    csv[t] = *(const f32x4*)(ws + CS_OFF + (((j * 4 + w) * 3 + t) * 16 + 4 * g) * 4);
    bsv[t] = *(const f32x4*)(ws + BS_OFF + (((j * 4 + w) * 3 + t) * 16 + 4 * g) * 4);
  }
  __builtin_amdgcn_sched_barrier(0);
  if (tid < 128) {
    float s = 0.f, q = 0.f;
#pragma unroll
    for (int i = 0; i < 5; ++i) { s += st[i][0] + st[i][2]; q += st[i][1] + st[i][3]; }
    const float m = s * (1.f / 640.f);
    const float var = fmaxf(q * (1.f / 640.f) - m * m, 0.f);
    const float sd = rbf(sqrtf(rbf(rbf(var) + 1e-5f)));
    *(f32x2*)(sm + 65536 + tid * 8) = f32x2{rbf(m), sd};
  }
  char* lw = sm + (2 * w) * 1024 + l * 16;
  const char* lr = sm + l * 16;
  *(u32x4*)(lw) = xf[0][0]; *(u32x4*)(lw + 1024) = xf[0][1];
  *(u32x4*)(lw + 8192) = xf[1][0]; *(u32x4*)(lw + 8192 + 1024) = xf[1][1];
  __syncthreads();
  f32x4 acc[8][3];
#pragma unroll
  for (int tt = 0; tt < 8; ++tt)
#pragma unroll
    for (int t = 0; t < 3; ++t) acc[tt][t] = f32x4{0.f, 0.f, 0.f, 0.f};
  u32x4 bf[2][8];
#pragma unroll
  for (int tt = 0; tt < 8; ++tt) bf[0][tt] = *(const u32x4*)(lr + tt * 1024);
#pragma unroll
  for (int k = 0; k < 20; ++k) {
    if (k + G1_PF < 20) {
      xf[k + G1_PF][0] = Xp[(k + G1_PF) * 512]; xf[k + G1_PF][1] = Xp[(k + G1_PF) * 512 + 64];
#pragma unroll
      for (int t = 0; t < 3; ++t) wf[k + G1_PF][t] = Wp[((k + G1_PF) * 3 + t) * 64];
    }
    if (k + 1 < 20) {
#pragma unroll
      for (int tt = 0; tt < 8; ++tt) bf[(k + 1) & 1][tt] = *(const u32x4*)(lr + ((k + 1) & 7) * 8192 + tt * 1024);
    }
#pragma unroll
    for (int tt = 0; tt < 8; ++tt)
#pragma unroll
      for (int t = 0; t < 3; ++t) acc[tt][t] = mfma16(wf[k][t], bf[k & 1][tt], acc[tt][t]);
    if (k + 2 < 20) {
      *(u32x4*)(lw + ((k + 2) & 7) * 8192) = xf[k + 2][0];
      *(u32x4*)(lw + ((k + 2) & 7) * 8192 + 1024) = xf[k + 2][1];
      __syncthreads();
    }
    __builtin_amdgcn_sched_barrier(0);
  }
  u32* HH = (u32*)(ws + HH_OFF) + (size_t)(s2 * 48 + 3 * j) * 8 * 256 + (16 * w + ln) * 4 + g;
  const float C0 = -2.f * 0.7978845608028654f * 1.4426950408889634f, C1 = C0 * 0.044715f;
#pragma unroll
  for (int tt = 0; tt < 8; ++tt) {
    const f32x2 ms = *(const f32x2*)(sm + 65536 + (16 * tt + ln) * 8);
    const float m = ms[0], sd = ms[1], r = 1.f / sd;
    const float c0 = C0 * r, c1 = C1 * r * r * r, ir2 = sd * sd;
#pragma unroll
    for (int t = 0; t < 3; ++t) {
      float a[4];
#pragma unroll
      for (int i = 0; i < 4; ++i) a[i] = fmaf(sd, bsv[t][i], fmaf(-m, csv[t][i], acc[tt][t][i]));
      float o[2];
#pragma unroll
      for (int i = 0; i < 2; ++i) {
        const float ag = a[i], au = a[2 + i];
        const float e = __builtin_amdgcn_exp2f(ag * fmaf(ag * ag, c1, c0));
        o[i] = ag * au * __builtin_amdgcn_rcpf(fmaf(e, ir2, ir2));
      }
      HH[(t * 8 + tt) * 256] = pk2(o[0], o[1]);
    }
  }
}

constexpr int G2_PF = 4;
__global__ __launch_bounds__(256, 1) void k5_gemm2(char* __restrict__ ws, u16* __restrict__ out) {
  __shared__ __attribute__((aligned(16))) char sm[81920];
  const int tid = threadIdx.x, xcd = blockIdx.x & 7, idx = blockIdx.x >> 3;
  const int th = idx & 7, s2 = 4 * (xcd >> 1) + (th >> 1), hf = th & 1, cgrp = 4 * (xcd & 1) + (idx >> 3);
  const int w = __builtin_amdgcn_readfirstlane(tid >> 6), l = tid & 63;
  const u32x4* Wp = (const u32x4*)(ws + WD_OFF) + (size_t)(cgrp * 48 + 12 * w) * 5 * 64 + l;
  const u32x4* Hp = (const u32x4*)(ws + HH_OFF) + (size_t)((s2 * 48 + 12 * w) * 8 + 4 * hf) * 64 + l;
  u32x4 wf[12][5], hv[12][4];
#pragma unroll
  for (int k = 0; k < G2_PF; ++k) {
#pragma unroll
    for (int t = 0; t < 4; ++t) hv[k][t] = Hp[(k * 8 + t) * 64];
#pragma unroll
    for (int t = 0; t < 5; ++t) wf[k][t] = Wp[(k * 5 + t) * 64];
  }
  __builtin_amdgcn_sched_barrier(0);
  f32x4 acc[4][5];
#pragma unroll
  for (int tt = 0; tt < 4; ++tt)
#pragma unroll
    for (int t = 0; t < 5; ++t) acc[tt][t] = f32x4{0.f, 0.f, 0.f, 0.f};
#pragma unroll
  for (int k = 0; k < 12; ++k) {
#pragma unroll
    for (int tt = 0; tt < 4; ++tt)
#pragma unroll
      for (int t = 0; t < 5; ++t) acc[tt][t] = mfma16(wf[k][t], hv[k][tt], acc[tt][t]);
    if (k + G2_PF < 12) {
#pragma unroll
      for (int t = 0; t < 4; ++t) hv[k + G2_PF][t] = Hp[((k + G2_PF) * 8 + t) * 64];
#pragma unroll
      for (int t = 0; t < 5; ++t) wf[k + G2_PF][t] = Wp[((k + G2_PF) * 5 + t) * 64];
    }
    __builtin_amdgcn_sched_barrier(0);
  }
  const int ln = l & 15, g = l >> 4;
  const int tt8 = 4 * hf + w;
  u32x2 xr[5];
  f32x4 gbv[5];
#pragma unroll
  for (int t = 0; t < 5; ++t) {
    const int c = 80 * cgrp + 16 * t + 4 * g;
    xr[t] = *(const u32x2*)(ws + X1_OFF + ((size_t)((s2 * 20 + (c >> 5)) * 8 + tt8) * 64 + ln + 16 * ((c & 15) >> 2)) * 16 + 8 * ((c >> 4) & 1));
    gbv[t] = *(const f32x4*)(ws + MOD_OFF + (3200 + c) * 4);
  }
#pragma unroll
  for (int tt = 0; tt < 4; ++tt)
    if (tt != w) {
#pragma unroll
      for (int t = 0; t < 5; ++t) *(f32x4*)(sm + ((w * 4 + tt) * 5 + t) * 1024 + l * 16) = acc[tt][t];
    }
  __syncthreads();
  f32x4 own[5];
#pragma unroll
  for (int t = 0; t < 5; ++t) own[t] = w == 0 ? acc[0][t] : w == 1 ? acc[1][t] : w == 2 ? acc[2][t] : acc[3][t];
#pragma unroll
  for (int d = 1; d < 4; ++d) {
    const int sw = (w + d) & 3;
#pragma unroll
    for (int t = 0; t < 5; ++t) own[t] += *(const f32x4*)(sm + ((sw * 4 + w) * 5 + t) * 1024 + l * 16);
  }
  const int s1 = 16 * tt8 + ln;
  u16* orow = out + (size_t)(16 * s1 + s2) * D_ + 80 * cgrp + 4 * g;
#pragma unroll
  for (int t = 0; t < 5; ++t) {
    float o[4];
#pragma unroll
    for (int r = 0; r < 4; ++r) {
      const float xx = (r & 1) ? hi2f(xr[t][r >> 1]) : lo2f(xr[t][r >> 1]);
      o[r] = xx + rbf(gbv[t][r] * rbf(own[t][r]));
    }
    *(u32x2*)(orow + 16 * t) = u32x2{pk2(o[0], o[1]), pk2(o[2], o[3])};
  }
}

extern "C" void kernel_launch(void* const* d_in, const int* in_sizes, int n_in, void* d_out, int out_size, void* d_ws,
                              size_t ws_size, hipStream_t stream) {
  const u16* x = (const u16*)d_in[0];
  const u16* wr = (const u16*)d_in[1];
  const u16* wi = (const u16*)d_in[2];
  const u16* wg = (const u16*)d_in[4];
  const u16* wu = (const u16*)d_in[5];
  const u16* wd = (const u16*)d_in[6];
  const u16* c = (const u16*)d_in[7];
  const u16* wmod = (const u16*)d_in[8];
  char* ws = (char*)d_ws;
  k1_ln_dft128<<<256, 512, 0, stream>>>(x, c, wmod, ws);
  k2_spec<<<240, 512, 0, stream>>>(wr, wi, wg, wu, ws);
  k3_idft128<<<256, 512, 0, stream>>>(x, wd, ws);
  k4_gemm1<<<256, 256, 0, stream>>>(ws);
  k5_gemm2<<<256, 256, 0, stream>>>(ws, (u16*)d_out);
}
```

```cpp
#include <hip/hip_runtime.h>

typedef unsigned short u16;
typedef unsigned int u32;
typedef __bf16 bf16x8 __attribute__((ext_vector_type(8)));
typedef __bf16 bf16x2 __attribute__((ext_vector_type(2)));
typedef float f32x2 __attribute__((ext_vector_type(2)));
typedef float f32x4 __attribute__((ext_vector_type(4)));
typedef float f32x16 __attribute__((ext_vector_type(16)));
typedef u32 u32x2 __attribute__((ext_vector_type(2)));
typedef u32 u32x4 __attribute__((ext_vector_type(4)));
typedef short s16x4 __attribute__((ext_vector_type(4)));
typedef __amdgpu_buffer_rsrc_t rsrc_t;
#define LAS __attribute__((address_space(3)))
#define DI __device__ __forceinline__
#define SB() __builtin_amdgcn_sched_barrier(0)

constexpr int D_ = 640, FF_ = 1536;
constexpr int MOD_OFF = 0;
constexpr int CS_OFF = 16384;
constexpr int STAT_OFF = 65536;
constexpr int MARK_OFF = STAT_OFF + 163840;
constexpr int U_OFF = 262144;
constexpr int UP_OFF = U_OFF + 2621440;
constexpr int X1_OFF = UP_OFF + 2662400;
constexpr int WP_OFF = X1_OFF + 2621440;
constexpr int WD_OFF = WP_OFF + 3932160;
constexpr int HH_OFF = WD_OFF + 1966080;
constexpr int FP_OFF = HH_OFF + 6291456;
constexpr int WS_USED = FP_OFF + 2662400;
constexpr u32 SENT = 0xffffffffu;

DI u32 pk2(float a, float b) {
  f32x2 v = {a, b};
  return __builtin_bit_cast(u32, __builtin_convertvector(v, bf16x2));
}
DI u32 pk2v(f32x2 v) { return __builtin_bit_cast(u32, __builtin_convertvector(v, bf16x2)); }
DI float rbf(float a) { return __builtin_bit_cast(float, pk2(a, 0.f) << 16); }
DI float u2f(u32 v) { return __builtin_bit_cast(float, v); }
DI u32 f2u(float v) { return __builtin_bit_cast(u32, v); }
DI float lo2f(u32 v) { return __builtin_bit_cast(float, v << 16); }
DI float hi2f(u32 v) { return __builtin_bit_cast(float, v & 0xffff0000u); }
DI f32x2 lo2(const f32x4& v) { return f32x2{v[0], v[1]}; }
DI f32x2 hi2(const f32x4& v) { return f32x2{v[2], v[3]}; }
DI f32x2 fma2(f32x2 a, f32x2 b, f32x2 c) { return __builtin_elementwise_fma(a, b, c); }
DI f32x4 mfma16(u32x4 a, u32x4 b, f32x4 c) {
  return __builtin_amdgcn_mfma_f32_16x16x32_bf16(__builtin_bit_cast(bf16x8, a), __builtin_bit_cast(bf16x8, b), c, 0, 0, 0);
}
DI f32x16 mfma32(u32x4 a, u32x4 b, f32x16 c) {
  return __builtin_amdgcn_mfma_f32_32x32x16_bf16(__builtin_bit_cast(bf16x8, a), __builtin_bit_cast(bf16x8, b), c, 0, 0, 0);
}
DI u32x2 trread(const char* p) {
  s16x4 r = __builtin_amdgcn_ds_read_tr16_b64_v4i16((LAS s16x4*)(p));
  return __builtin_bit_cast(u32x2, r);
}
DI u32x4 trread2(const char* p, int d) {
  const u32x2 a = trread(p), b = trread(p + d);
  return u32x4{a[0], a[1], b[0], b[1]};
}
DI float cosr(float rev) { return __builtin_amdgcn_cosf(__builtin_amdgcn_fractf(rev)); }
DI float dot2(u32 a, u32 b, float c) {
  return __builtin_amdgcn_fdot2_f32_bf16(__builtin_bit_cast(bf16x2, a), __builtin_bit_cast(bf16x2, b), c, false);
}
template <int CTRL> DI float dppf(float v) {
  return __builtin_bit_cast(float, __builtin_amdgcn_update_dpp(0, __builtin_bit_cast(int, v), CTRL, 0xf, 0xf, true));
}
DI rsrc_t mkrs(const void* p, int bytes) { return __builtin_amdgcn_make_buffer_rsrc((void*)p, 0, bytes, 0x00020000); }
DI u32x4 ld128(rsrc_t r, int v, int s) { return __builtin_amdgcn_raw_buffer_load_b128(r, v, s, 0); }
DI u32x2 ld64(rsrc_t r, int v, int s) { return __builtin_amdgcn_raw_buffer_load_b64(r, v, s, 0); }
DI u32 ld32(rsrc_t r, int v, int s) { return __builtin_amdgcn_raw_buffer_load_b32(r, v, s, 0); }
DI f32x4 ldf4(rsrc_t r, int v, int s) { return __builtin_bit_cast(f32x4, ld128(r, v, s)); }
DI void st128(u32x4 d, rsrc_t r, int v, int s) { __builtin_amdgcn_raw_buffer_store_b128(d, r, v + s, 0, 0); }
DI u32x4 ld128c(rsrc_t r, int v, int s) { return __builtin_amdgcn_raw_buffer_load_b128(r, v, s, 16); }
DI void st128c(u32x4 d, rsrc_t r, int v, int s) { __builtin_amdgcn_raw_buffer_store_b128(d, r, v + s, 0, 16); }
DI void st64c(u32x2 d, rsrc_t r, int v, int s) { __builtin_amdgcn_raw_buffer_store_b64(d, r, v, s, 16); }
DI void st64(u32x2 d, rsrc_t r, int v, int s) { __builtin_amdgcn_raw_buffer_store_b64(d, r, v, s, 0); }
DI void st32(u32 d, rsrc_t r, int v, int s) { __builtin_amdgcn_raw_buffer_store_b32(d, r, v, s, 0); }
DI void pin(u32x4& v) { asm volatile("" : "+v"(v)); }
DI void bar() { asm volatile("s_waitcnt lgkmcnt(0)" ::: "memory"); __builtin_amdgcn_s_barrier(); asm volatile("" ::: "memory"); }
DI void ln_scalars(float s, float q, float& mb, float& sd) {
  const float m = s * (1.f / 640.f);
  const float var = fmaxf(fmaf(-m, m, q * (1.f / 640.f)), 0.f);
  mb = rbf(m);
  sd = rbf(__builtin_amdgcn_sqrtf(rbf(rbf(var) + 1e-5f)));
}

#define STAMP(K, I)
#define STAMPW(K, I, W)

DI u32x4 touch_code(int lane) {
  unsigned long long pc;
  int o1, o2;
  asm volatile("s_getpc_b64 %0\n1:\n\ts_mov_b32 %1, k2_spec-1b\n\ts_mov_b32 %2, k9_end-1b" : "=s"(pc), "=s"(o1), "=s"(o2));
  const rsrc_t rc = mkrs((const void*)(pc + (long long)o1), o2 - o1);
  return u32x4{ld32(rc, lane * 128, 0), ld32(rc, lane * 128 + 8192, 0), ld32(rc, lane * 128 + 16384, 0), ld32(rc, lane * 128 + 24576, 0)};
}

extern "C" __global__ __launch_bounds__(512, 2) void k1_ln_dft128(const u16* __restrict__ x, const u16* __restrict__ cvec,
                                                                   const u16* __restrict__ wmod, const u16* __restrict__ wr_,
                                                                   const u16* __restrict__ wi_, char* __restrict__ ws) {
  __shared__ __attribute__((aligned(16))) char sm[13 * 2688 + 64];
  const int tid = threadIdx.x, xcd = blockIdx.x & 7, idx = blockIdx.x >> 3;
  const int w = __builtin_amdgcn_readfirstlane(tid >> 6), l = tid & 63;
  const rsrc_t rw = mkrs(ws, WS_USED);
  STAMP(0, 0);
  if (__builtin_expect(idx < 10, 1)) {
    const int s2 = 2 * xcd + idx / 5, cg = idx % 5;
    const int j8 = tid & 7, rr = tid >> 3;
    const rsrc_t rx = mkrs(x, 2048 * 640 * 2);
    u32x4 v[2][10];
    const int vo = (16 * rr + s2) * 1280 + j8 * 16;
#define XCH(i) ((2 * cg + (i) >= 10 ? 2 * cg + (i) - 10 : 2 * cg + (i)) * 128)
#pragma unroll
    for (int i = 0; i < 10; ++i) v[0][i] = ld128(rx, vo, XCH(i));
    SB();
    const int h = l >> 5, Mt = w & 3, nh = w >> 2;
    u32x4 ac[8];
    {
      const int row = l & 31, j = 16 * Mt + (row >> 1), part = row & 1;
      const int jj = (part && j == 0) ? 64 : j;
      const float step = (float)jj * (1.f / 128.f);
      const float a0 = fmaf(step, (float)(8 * h), (part && j != 0) ? 0.25f : 0.f);
      f32x2 c0[4], c1[4];
#pragma unroll
      for (int e = 0; e < 8; ++e) {
        c0[e >> 1][e & 1] = __builtin_amdgcn_cosf(fmaf(step, (float)e, a0));
        c1[e >> 1][e & 1] = __builtin_amdgcn_cosf(fmaf(step, (float)(16 + e), a0));
      }
      const float t16 = 2.f * __builtin_amdgcn_cosf(step * 16.f);
#pragma unroll
      for (int kap = 0; kap < 8; ++kap) {
#pragma unroll
        for (int e = 0; e < 4; ++e) ac[kap][e] = pk2v(c0[e]);
        pin(ac[kap]);
        if (kap < 7) {
#pragma unroll
          for (int e = 0; e < 4; ++e) { const f32x2 n = fma2(f32x2{t16, t16}, c1[e], -c0[e]); c0[e] = c1[e]; c1[e] = n; }
        }
      }
    }
    SB();
    STAMP(0, 1);
    f32x16 acc[2];
#pragma unroll
    for (int i = 0; i < 16; ++i) { acc[0][i] = 0.f; acc[1][i] = 0.f; }
    const int blk = (l >> 4) & 1, q4 = (l & 15) >> 2, p4 = l & 3;
    const char* rb0 = sm + (8 * h + q4) * 256 + (((2 * nh) ^ q4) << 6) + 32 * blk + 8 * p4;
    const char* rb1 = sm + (8 * h + q4) * 256 + (((2 * nh + 1) ^ q4) << 6) + 32 * blk + 8 * p4;
    char* wb0 = sm + rr * 256 + ((((j8 >> 2)) ^ (rr & 3)) << 6) + (j8 & 3) * 16;
    char* wb1 = sm + rr * 256 + (((2 + (j8 >> 2)) ^ (rr & 3)) << 6) + (j8 & 3) * 16;
#pragma unroll
    for (int ps = 0; ps < 2; ++ps) {
      float s = 0.f, q = 0.f, s_ = 0.f, q_ = 0.f;
#pragma unroll
      for (int i = 0; i < 10; ++i) {
        if (ps == 0) v[1][i] = ld128(rx, vo, 64 * 16 * 1280 + XCH(i));
        s = dot2(v[ps][i][0], 0x3f803f80u, s); q = dot2(v[ps][i][0], v[ps][i][0], q);
        s_ = dot2(v[ps][i][1], 0x3f803f80u, s_); q_ = dot2(v[ps][i][1], v[ps][i][1], q_);
        s = dot2(v[ps][i][2], 0x3f803f80u, s); q = dot2(v[ps][i][2], v[ps][i][2], q);
        s_ = dot2(v[ps][i][3], 0x3f803f80u, s_); q_ = dot2(v[ps][i][3], v[ps][i][3], q_);
        SB();
      }
      s += s_; q += q_;
      s += dppf<0xB1>(s); q += dppf<0xB1>(q);
      s += dppf<0x4E>(s); q += dppf<0x4E>(q);
      s += dppf<0x141>(s); q += dppf<0x141>(q);
      float mb, sd;
      ln_scalars(s, q, mb, sd);
      const float r = __builtin_amdgcn_rcpf(sd), mr = -mb * r;
#pragma unroll
      for (int c = 0; c < 2; ++c) {
        u32x4 o;
#pragma unroll
        for (int e = 0; e < 4; ++e) o[e] = pk2v(fma2(f32x2{lo2f(v[ps][c][e]), hi2f(v[ps][c][e])}, f32x2{r, r}, f32x2{mr, mr}));
        *(u32x4*)((c ? wb1 : wb0) + ps * 16384) = o;
      }
      bar();
#pragma unroll
      for (int kk = 0; kk < 4; ++kk) {
        acc[0] = mfma32(ac[4 * ps + kk], trread2(rb0 + (4 * ps + kk) * 4096, 1024), acc[0]);
        acc[1] = mfma32(ac[4 * ps + kk], trread2(rb1 + (4 * ps + kk) * 4096, 1024), acc[1]);
      }
      SB();
      if (ps == 0) STAMP(0, 2);
    }
    STAMP(0, 4);
    const int so = ((16 * Mt + 2 * h) * 16 + s2) * 2560 + (128 * cg + 64 * nh + (l & 31)) * 4;
#pragma unroll
    for (int nt = 0; nt < 2; ++nt)
#pragma unroll
      for (int i = 0; i < 16; i += 2) st32(pk2(acc[nt][i], acc[nt][i + 1]), rw, so, U_OFF + (((i & 3) >> 1) + 4 * (i >> 2)) * 16 * 2560 + nt * 128);
  } else if (idx < 22) {
    const int id = xcd * 12 + (idx - 10);
    const rsrc_t rm = mkrs(wmod, 640 * 3840 * 2), rc = mkrs(cvec, 640 * 2);
    const int rs = tid / 5, j5 = tid - rs * 5;
    float* red = (float*)sm;
    u32x4 tc = {0u, 0u, 0u, 0u};
    if (w < 5) {
      u32x4 wv[10];
      u32 cv[10];
#pragma unroll
      for (int i = 0; i < 10; ++i) cv[i] = __builtin_amdgcn_raw_buffer_load_b16(rc, rs * 2, i * 128, 0);
#pragma unroll
      for (int i = 0; i < 10; ++i) wv[i] = ld128(rm, rs * 7680 + id * 80 + j5 * 16, i * 64 * 7680);
      SB();
#pragma unroll
      for (int i = 0; i < 10; ++i) {
        const float c = lo2f(cv[i]);
        cv[i] = pk2(c * __builtin_amdgcn_rcpf(1.f + __expf(-c)), 0.f);
      }
      float a[8];
#pragma unroll
      for (int e = 0; e < 8; ++e) a[e] = 0.f;
#pragma unroll
      for (int i = 0; i < 10; ++i) {
        const u32 shi = cv[i] << 16;
#pragma unroll
        for (int e = 0; e < 4; ++e) { a[2 * e] = dot2(wv[i][e], cv[i], a[2 * e]); a[2 * e + 1] = dot2(wv[i][e], shi, a[2 * e + 1]); }
      }
      *(f32x4*)(red + rs * 40 + j5 * 8) = f32x4{a[0], a[1], a[2], a[3]};
      *(f32x4*)(red + rs * 40 + j5 * 8 + 4) = f32x4{a[4], a[5], a[6], a[7]};
    } else if (w == 7 && idx == 10) {
      tc = touch_code(l);
    }
    bar();
    if (tid < 320) {
      const int col = tid % 40, part = tid / 40;
      float s = 0.f;
#pragma unroll
      for (int i = 0; i < 8; ++i) s += red[(part * 8 + i) * 40 + col];
      red[2560 + part * 40 + col] = s;
    }
    bar();
    if (tid < 40) {
      float s = 0.f;
#pragma unroll
      for (int i = 0; i < 8; ++i) s += red[2560 + i * 40 + tid];
      s = rbf(s);
      const int col = 40 * id + tid, ch = col / 640;
      if (ch == 1 || ch == 4) s = rbf(1.f + s);
      if (ch == 2) s += s;
      ((float*)(ws + MOD_OFF))[col] = s;
    }
    pin(tc);
  } else {
    const int id = xcd * 10 + (idx - 22);
    const rsrc_t rr_ = mkrs(wr_, 2048 * 321 * 2), ri_ = mkrs(wi_, 2048 * 321 * 2);
    u32x4 fch[6];
    int fdst[3];
#pragma unroll
    for (int i = 0; i < 3; ++i) {
      const int n = tid + 512 * i, pc = n / 42, ch = n - pc * 42;
      const int unit = id * 13 + (pc >> 1), ks = (unit >> 4) + 128 * (unit & 15), row = (pc & 1) ? ((2048 - ks) & 2047) : ks;
      const int off = n < 1092 ? ((row * 642) & ~15) + ch * 16 : 0x7ffffff0;
      fch[2 * i] = ld128(rr_, off, 0);
      fch[2 * i + 1] = ld128(ri_, off, 0);
      fdst[i] = pc * 1344 + ch * 16;
    }
    SB();
#pragma unroll
    for (int i = 0; i < 3; ++i)
      if (i < 2 || tid < 68) { *(u32x4*)(sm + fdst[i]) = fch[2 * i]; *(u32x4*)(sm + fdst[i] + 672) = fch[2 * i + 1]; }
    bar();
#pragma unroll 1
    for (int q = w; q < 39; q += 8) {
      const int ul = q / 3, kind = q - 3 * ul, unit = id * 13 + ul;
      const int k1 = unit >> 4, rho = unit & 15, ks = k1 + 128 * rho;
      const int ln = l & 15, p = kind < 2 ? kind : ((l >> 4) & 1), g = kind < 2 ? l >> 4 : 4;
      const int kd0 = 2 * ln + p + 128 * g;
      const char* bd = sm + ul * 2688 + 2 * (ks & 7) + 2 * kd0;
      const char* bm = sm + ul * 2688 + 1344 + 2 * ((-ks) & 7) + 1280 - 2 * kd0;
      u32x4 o;
#pragma unroll
      for (int r = 0; r < 4; ++r) {
        const bool dir = kd0 + 32 * r <= 320;
        const char* src = dir ? bd + 64 * r : bm - 64 * r;
        const u32 a = *(const u16*)src, b = *(const u16*)(src + 672);
        o[r] = (a | (b << 16)) ^ (dir ? 0u : 0x80000000u);
      }
      if (kind == 0 && (l & 31) == 0) {
        const char* src = sm + ul * 2688 + 1344 + 2 * ((-ks) & 7) + (l ? 640 : 0);
        const u32 a2 = *(const u16*)src, b2 = *(const u16*)(src + 672), cur = l ? o[2] : o[0];
        const u32 sym = pk2(0.5f * (lo2f(cur) + lo2f(a2)), 0.5f * (hi2f(cur) - lo2f(b2)));
        if (l) o[2] = sym; else o[0] = sym;
      }
      const int dst = ((k1 * 2 + p) * 8 + (rho >> 1)) * 2560 + (kind < 2 ? (rho & 1) * 1024 + l * 16 : 2048 + ((rho & 1) * 16 + ln) * 16);
      if (kind < 2 || l < 32) st128(o, rw, dst, FP_OFF);
    }
  }
  STAMP(0, 5);
}

constexpr int VP = 1320;
constexpr int CT_OFF = 21504;
enum { SL_D1 = 0, SL_MM = 2, SL_ME = 4, SL_EM = 8, SL_EE = 12, SL_D1I = 14, SL_CI = 16, SL_TFM = 18, SL_TFE = 20, SL_TIM = 22, SL_TIE = 24 };

DI u32x4 twpack(const f32x4& re, const f32x4& im, const f32x4& c, const f32x4& s) {
  const f32x2 a0 = fma2(lo2(im), lo2(s), lo2(re) * lo2(c)), a1 = fma2(hi2(im), hi2(s), hi2(re) * hi2(c));
  const f32x2 b0 = fma2(-lo2(re), lo2(s), lo2(im) * lo2(c)), b1 = fma2(-hi2(re), hi2(s), hi2(im) * hi2(c));
  return u32x4{pk2v(a0), pk2v(a1), pk2v(b0), pk2v(b1)};
}
DI u32x4 fltpack(const f32x4& re, const f32x4& im, const f32x4& c, const f32x4& s) {
  const f32x2 a0 = fma2(-lo2(im), lo2(s), lo2(re) * lo2(c)), a1 = fma2(-hi2(im), hi2(s), hi2(re) * hi2(c));
  const f32x2 b0 = fma2(-lo2(re), lo2(s), -lo2(im) * lo2(c)), b1 = fma2(-hi2(re), hi2(s), -hi2(im) * hi2(c));
  return u32x4{pk2v(a0), pk2v(a1), pk2v(b0), pk2v(b1)};
}

extern "C" __global__ __launch_bounds__(512, 2) void k2_spec(const u16* __restrict__ wg, const u16* __restrict__ wu,
                                                  const u16* __restrict__ wd, char* __restrict__ ws) {
  __shared__ __attribute__((aligned(16))) char sm[CT_OFF + 26 * 1024];
  const int tid = threadIdx.x, xcd = blockIdx.x & 7, idx = blockIdx.x >> 3;
  const rsrc_t rw = mkrs(ws, WS_USED);
  STAMP(1, 0);
  if (idx < 16 || (idx < 18 && xcd == 0)) {
    const int k1 = xcd + 8 * (idx >> 1), p = idx & 1;
    const int w = __builtin_amdgcn_readfirstlane(tid >> 6), l = tid & 63, ln = l & 15, g = l >> 4;
    u32x4 ua[3][2];
    f32x4 cav[3][2], sav[3][2];
    u32x4 fw[3];
    {
      const int ub = U_OFF + (k1 & 63) * 16 * 2560 + w * 64;
      const int vo = 4 * g * 2560 + ln * 4;
      const int u2 = w < 4 ? 1024 : 0;
#pragma unroll
      for (int t = 0; t < 3; ++t)
#pragma unroll
        for (int hf = 0; hf < 2; ++hf)
#pragma unroll
          for (int i = 0; i < 4; ++i) ua[t][hf][i] = ld32(rw, vo, ub + (t < 2 ? t * 512 : u2) + hf * 1280 + i * 2560);
#pragma unroll
      for (int t = 0; t < 3; ++t)
#pragma unroll
        for (int hf = 0; hf < 2; ++hf) cav[t][hf] = ldf4(rw, g * 16, MOD_OFF + 2560 + w * 64 + (t < 2 ? t * 512 : u2) + hf * 1280);
      const int fb = FP_OFF + ((k1 * 2 + p) * 8 + w) * 2560;
      fw[0] = ld128(rw, l * 16, fb);
      fw[1] = ld128(rw, l * 16, fb + 1024);
      fw[2] = ld128(rw, (l & 31) * 16, fb + 2048);
      if (k1 == 0) {
#pragma unroll
        for (int t = 0; t < 3; ++t)
#pragma unroll
          for (int hf = 0; hf < 2; ++hf) sav[t][hf] = ldf4(rw, g * 16, MOD_OFF + w * 64 + (t < 2 ? t * 512 : u2) + hf * 1280);
      }
    }
    SB();
    STAMP(1, 1);
    u32x4 cre, cim;
    {
      const float step = (float)(k1 + 128 * ln) * (1.f / 2048.f);
      const u32 mlo = k1 == 64 ? 0u : 0xffffu, mhi = k1 == 0 ? 0u : 0xffff0000u;
#pragma unroll
      for (int i = 0; i < 4; ++i) {
        const float a = __builtin_amdgcn_fractf(step * (float)(4 * g + i));
        const float c = __builtin_amdgcn_cosf(a), s = __builtin_amdgcn_sinf(a);
        cre[i] = pk2(c, k1 == 64 ? c : s) & (mlo | mhi);
        cim[i] = pk2(-s, k1 == 64 ? -s : c) & (mlo | mhi);
      }
    }
    char* ct = sm + CT_OFF + l * 16;
#pragma unroll 1
    for (int set = w; set < 11; set += (w >= 4 ? 4 : 16)) {
      const float fln = (float)ln, fg4 = (float)(4 * g), fp = (float)p, fe = (float)(16 + (ln & 3));
      const bool ext = set == 3 || set == 4 || set == 10;
      const bool odd = set == 0 || set == 7 || set == 8;
      const float A = odd ? 2.f * fln + fp : ext ? fe : fln;
      const bool in16 = set == 2 || set == 4 || set == 8;
      const bool dbl = set == 5 || set >= 9;
      const float B = set == 6 ? fmaf(fg4, 128.f, (float)k1) : dbl ? 2.f * fg4 + fp : in16 ? 16.f : fg4;
      const float C = set == 6 ? 128.f : dbl ? 2.f : 1.f;
      const float Dn = set == 6 ? (1.f / 2048.f) : (set == 0 || set == 5) ? (1.f / 32.f) : set >= 7 ? (1.f / 640.f) : (1.f / 20.f);
      const float base = A * B * Dn, step = A * C * Dn;
      f32x4 cs, sn;
#pragma unroll
      for (int e = 0; e < 4; ++e) {
        const float a = __builtin_amdgcn_fractf(fmaf(step, (float)e, base));
        cs[e] = __builtin_amdgcn_cosf(a);
        sn[e] = __builtin_amdgcn_sinf(a);
      }
      if (set >= 7) {
        const float sc = set >= 9 ? (1.f / (2048.f * 640.f)) : 1.f;
        const int sl = SL_TFM + 2 * (set - 7);
        *(f32x4*)(ct + sl * 1024) = cs * sc;
        *(f32x4*)(ct + (sl + 1) * 1024) = sn * sc;
      } else {
        const u32 c01 = pk2(cs[0], cs[1]), c23 = pk2(cs[2], cs[3]), s01 = pk2(sn[0], sn[1]), s23 = pk2(sn[2], sn[3]);
        const u32x4 fre = {c01, c23, s01, s23}, fim = {s01 ^ 0x80008000u, s23 ^ 0x80008000u, c01, c23};
        const u32x4 z = {0u, 0u, 0u, 0u};
        const int sel = set == 2 ? g : (ln >> 2);
        const bool two = set == 2 || set == 3;
        const bool keep = two ? sel == 0 : (set != 4 || (sel == g && g < 2));
        const int sl = set <= 2 ? 2 * set : set == 3 ? SL_EM : 2 * set + 4;
        *(u32x4*)(ct + sl * 1024) = keep ? fre : z;
        *(u32x4*)(ct + (sl + 1) * 1024) = keep ? fim : z;
        if (two) {
          *(u32x4*)(ct + (sl + 2) * 1024) = sel == 1 ? fre : z;
          *(u32x4*)(ct + (sl + 3) * 1024) = sel == 1 ? fim : z;
        }
      }
    }
    STAMP(1, 2);
    const f32x4 z4 = {0.f, 0.f, 0.f, 0.f};
    {
      const float sg = p ? -1.f : 1.f;
      const f32x2 sg2 = {sg, sg};
#pragma unroll
      for (int t = 0; t < 3; ++t) {
        if (t == 2 && w >= 4) break;
        const f32x4 r1 = mfma16(ua[t][0], cre, z4), i1 = mfma16(ua[t][0], cim, z4);
        const f32x4 r2 = mfma16(ua[t][1], cre, z4), i2 = mfma16(ua[t][1], cim, z4);
        const f32x2 c2l = lo2(cav[t][1]) * sg2, c2h = hi2(cav[t][1]) * sg2;
        f32x2 vr0 = fma2(lo2(r2), c2l, lo2(r1) * lo2(cav[t][0])), vr1 = fma2(hi2(r2), c2h, hi2(r1) * hi2(cav[t][0]));
        const f32x2 vi0 = fma2(lo2(i2), c2l, lo2(i1) * lo2(cav[t][0])), vi1 = fma2(hi2(i2), c2h, hi2(i1) * hi2(cav[t][0]));
        if (k1 == 0) {
          const float on = ln == 0 ? 2048.f : 0.f;
          vr0 += (lo2(sav[t][0]) + sg2 * lo2(sav[t][1])) * on;
          vr1 += (hi2(sav[t][0]) + sg2 * hi2(sav[t][1])) * on;
        }
        char* dst = sm + ln * VP + (16 * w + 4 * g) * 2 + t * 256;
        *(u32x2*)(dst) = u32x2{pk2v(vr0), pk2v(vr1)};
        *(u32x2*)(dst + 640) = u32x2{pk2v(vi0), pk2v(vi1)};
      }
    }
    STAMP(1, 3);
    bar();
    STAMP(1, 4);
#define CT(sl) (*(const u32x4*)(ct + (sl) * 1024))
#define CTF(sl) (*(const f32x4*)(ct + (sl) * 1024))
    {
      const int q = ln >> 2, pp = l & 3;
      u32x4 y[3];
      {
        const u32x4 bre = CT(SL_D1), bim = CT(SL_D1 + 1);
        const f32x4 tcm = CTF(SL_TFM), tsm = CTF(SL_TFM + 1), tce = CTF(SL_TFE), tse = CTF(SL_TFE + 1);
        const char* srcm = sm + 2 * w * VP + ((4 * g + q) * 20 + 4 * pp) * 2;
        const char* srce = sm + (2 * w + (pp & 1)) * VP + ((4 * g + q) * 20 + 16) * 2;
#pragma unroll
        for (int t = 0; t < 3; ++t) {
          const u32x4 a = trread2(t < 2 ? srcm + t * VP : srce, 640);
          const f32x4 re = mfma16(a, bre, z4), im = mfma16(a, bim, z4);
          y[t] = t < 2 ? twpack(re, im, tcm, tsm) : twpack(re, im, tce, tse);
        }
      }
      const u32x4 mm0 = CT(SL_MM), mm1 = CT(SL_MM + 1), ee0 = CT(SL_EE), ee1 = CT(SL_EE + 1);
      u32x4 me[4], em[4];
#pragma unroll
      for (int i = 0; i < 4; ++i) { me[i] = CT(SL_ME + i); em[i] = CT(SL_EM + i); }
      u32x4 yf[3];
      {
        f32x4 re[3], im[3];
#pragma unroll
        for (int v = 0; v < 2; ++v) { re[v] = mfma16(mm0, y[v], z4); im[v] = mfma16(mm1, y[v], z4); }
        re[2] = mfma16(em[2], y[1], mfma16(em[0], y[0], z4));
        im[2] = mfma16(em[3], y[1], mfma16(em[1], y[0], z4));
#pragma unroll
        for (int v = 0; v < 2; ++v) { re[v] = mfma16(me[2 * v], y[2], re[v]); im[v] = mfma16(me[2 * v + 1], y[2], im[v]); }
        re[2] = mfma16(ee0, y[2], re[2]);
        im[2] = mfma16(ee1, y[2], im[2]);
#pragma unroll
        for (int t = 0; t < 3; ++t) {
          const f32x4 fr = {lo2f(fw[t][0]), lo2f(fw[t][1]), lo2f(fw[t][2]), lo2f(fw[t][3])};
          const f32x4 fi = {hi2f(fw[t][0]), hi2f(fw[t][1]), hi2f(fw[t][2]), hi2f(fw[t][3])};
          yf[t] = fltpack(re[t], im[t], fr, fi);
        }
      }
      {
        f32x4 re[3], im[3];
#pragma unroll
        for (int v = 0; v < 2; ++v) { re[v] = mfma16(yf[v], mm0, z4); im[v] = mfma16(yf[v], mm1, z4); }
        re[2] = mfma16(yf[1], em[2], mfma16(yf[0], em[0], z4));
        im[2] = mfma16(yf[1], em[3], mfma16(yf[0], em[1], z4));
#pragma unroll
        for (int v = 0; v < 2; ++v) { re[v] = mfma16(yf[2], me[2 * v], re[v]); im[v] = mfma16(yf[2], me[2 * v + 1], im[v]); }
        re[2] = mfma16(yf[2], ee0, re[2]);
        im[2] = mfma16(yf[2], ee1, im[2]);
        const u32x4 bre = CT(SL_D1I), bim = CT(SL_D1I + 1);
        const f32x4 tcm = CTF(SL_TIM), tsm = CTF(SL_TIM + 1), tce = CTF(SL_TIE), tse = CTF(SL_TIE + 1);
        char* dstm = sm + 2 * w * VP + (20 * ln + 4 * g) * 2;
        char* dste = sm + (2 * w + (g & 1)) * VP + (20 * ln + 16) * 2;
#pragma unroll
        for (int t = 0; t < 3; ++t) {
          const u32x4 a = t < 2 ? twpack(re[t], im[t], tcm, tsm) : twpack(re[t], im[t], tce, tse);
          const f32x4 vr = mfma16(a, bre, z4), vi = mfma16(a, bim, z4);
          char* dst = t < 2 ? dstm + t * VP : dste;
          if (t < 2 || g < 2) {
            *(u32x2*)(dst) = u32x2{pk2(vr[0], vr[1]), pk2(vr[2], vr[3])};
            *(u32x2*)(dst + 640) = u32x2{pk2(vi[0], vi[1]), pk2(vi[2], vi[3])};
          }
        }
      }
    }
    STAMP(1, 5);
    const u32x4 bre = CT(SL_CI), bim = CT(SL_CI + 1);
    bar();
    STAMP(1, 6);
    {
      const int q = ln >> 2, pp = l & 3;
      const char* src = sm + (4 * g + q) * VP + (16 * w + 4 * pp) * 2;
      const int vo = ln * 1280 + g * 16, so = UP_OFF + (k1 * 2 + p) * 16 * 1280 + w * 64;
#pragma unroll
      for (int t = 0; t < 3; ++t) {
        if (t == 2 && w >= 4) break;
        const u32x4 a = trread2(src + t * 256, 640);
        const f32x4 re = mfma16(a, bre, z4), im = mfma16(a, bim, z4);
        st128(u32x4{pk2(re[0], im[0]), pk2(re[1], im[1]), pk2(re[2], im[2]), pk2(re[3], im[3])}, rw, vo, so + t * 512);
      }
    }
    if (blockIdx.x == 0) ((u32*)(ws + MARK_OFF))[tid] = SENT;
#undef CT
#undef CTF
  } else if (idx >= 18 && idx < 30) {
    const int id = xcd * 12 + (idx - 18);
    const float* modf = (const float*)(ws + MOD_OFF);
    float* red = (float*)sm;
    const int j = id / 6;
    if (tid < 320) {
      const int ng = tid & 3, kg = tid >> 2, mat = ng & 1, hf = ng >> 1;
      const int kstep = kg >> 2, g = kg & 3;
      const u16* W = (mat ? wu : wg) + 16 * id + 8 * hf + (size_t)(8 * kg) * FF_;
      u32x4 v[8];
#pragma unroll
      for (int e = 0; e < 8; ++e) v[e] = *(const u32x4*)(W + (size_t)e * FF_);
      const f32x4 f0 = *(const f32x4*)(modf + 2560 + 8 * kg), f1 = *(const f32x4*)(modf + 2560 + 8 * kg + 4);
      const f32x4 b0 = *(const f32x4*)(modf + 1920 + 8 * kg), b1 = *(const f32x4*)(modf + 1920 + 8 * kg + 4);
      const int ffl = (16 * id + 8 * hf) % 96, wv = ffl / 24, tile = (ffl % 24) >> 3;
      const int dst = (((((j * 4 + wv) * 20 + kstep) * 3 + tile) * 64 + 16 * g + 2 * mat)) * 16;
#pragma unroll
      for (int c = 0; c < 8; ++c) {
        f32x2 wp[4];
#pragma unroll
        for (int e = 0; e < 4; ++e)
          wp[e] = (c & 1) ? f32x2{hi2f(v[2 * e][c >> 1]), hi2f(v[2 * e + 1][c >> 1])} : f32x2{lo2f(v[2 * e][c >> 1]), lo2f(v[2 * e + 1][c >> 1])};
        const u32x4 o = {pk2v(wp[0] * lo2(f0)), pk2v(wp[1] * hi2(f0)), pk2v(wp[2] * lo2(f1)), pk2v(wp[3] * hi2(f1))};
        float cs = 0.f;
#pragma unroll
        for (int e = 0; e < 4; ++e) cs = dot2(o[e], 0x3f803f80u, cs);
        const f32x2 bs = fma2(wp[0], lo2(b0), fma2(wp[1], hi2(b0), fma2(wp[2], lo2(b1), wp[3] * hi2(b1))));
        st128(o, rw, dst + (4 * (c >> 1) + (c & 1)) * 16, WP_OFF);
        red[kg * 64 + ng * 8 + c] = cs;
        red[kg * 64 + 32 + ng * 8 + c] = bs[0] + bs[1];
      }
    }
    __syncthreads();
    {
      const int o = tid & 63, part = tid >> 6;
      float s = 0.f;
#pragma unroll
      for (int i = 0; i < 10; ++i) s += red[(part * 10 + i) * 64 + o];
      red[5120 + part * 64 + o] = s;
    }
    __syncthreads();
    if (tid < 64) {
      float s = 0.f;
#pragma unroll
      for (int i = 0; i < 8; ++i) s += red[5120 + i * 64 + tid];
      const int which = tid >> 5, ng = (tid >> 3) & 3, c = tid & 7, mat = ng & 1, hf = ng >> 1;
      const int ffl = (16 * id + 8 * hf) % 96, wv = ffl / 24, tile = (ffl % 24) >> 3;
      const float h0 = rbf(s), h1 = rbf(s - h0), h2 = (s - h0) - h1;
      st64(u32x2{pk2(h0, h1), pk2(h2, 0.f)}, rw, (((j * 4 + wv) * 3 + tile) * 16 + 4 * (c >> 1) + 2 * mat + (c & 1)) * 16 + which * 8, CS_OFF);
    }
  } else {
    const int id = idx >= 30 ? (idx - 30) * 8 + xcd : 16 + (idx - 16) * 7 + (xcd - 1);
    const int it = id * 512 + tid, fg = it / 80, cgp = it - fg * 80;
    u32x4 v[8];
#pragma unroll
    for (int e = 0; e < 8; ++e) v[e] = *(const u32x4*)(wd + (size_t)(8 * fg + e) * D_ + 8 * cgp);
    const int j = fg / 12, rem = fg % 12, gq = rem / 3, t = rem % 3;
    const int col = 8 * cgp, cgrp = col / 80, tile = (col % 80) >> 4;
    const int dst = (((cgrp * 48 + 3 * j + t) * 5 + tile) * 64 + 16 * gq + ((col % 80) & 15)) * 16;
#pragma unroll
    for (int c = 0; c < 8; ++c) {
      u32x4 o;
#pragma unroll
      for (int e = 0; e < 4; ++e)
        o[e] = __builtin_amdgcn_perm(v[2 * e + 1][c >> 1], v[2 * e][c >> 1], (c & 1) ? 0x07060302u : 0x05040100u);
      st128(o, rw, dst + c * 16, WD_OFF);
    }
  }
  STAMP(1, 7);
}

constexpr int L_TOK = 0, L_XB = 81920, L_ST = 131072, L_MS = 133120, L_END = 135168;
constexpr u32 SPIN_CAP = 1u << 20;
#define W_C 21
#define H2E 3
#define NWE 10
template <int V> struct IC { static constexpr int value = V; };
template <int I, int N, class F> DI void static_for(F& f) { if constexpr (I < N) { f(IC<I>{}); static_for<I + 1, N>(f); } }
DI u32 umax(u32 a, u32 b) { return a > b ? a : b; }
DI u32 umin(u32 a, u32 b) { return a < b ? a : b; }
DI u32 mx4(u32 m, const u32x4& v) { return umax(umax(umax(m, v[0]), v[1]), umax(v[2], v[3])); }
DI bool any_sent(u32 m) { return __builtin_amdgcn_ballot_w64(m == SENT) != 0ull; }
DI void dma1k(rsrc_t r, int voff, int soff, u32 lds) {
  u32 keep;
  asm volatile("s_mov_b32 %0, m0\n\ts_mov_b32 m0, %4\n\ts_nop 0\n\tbuffer_load_dwordx4 %1, %2, %3 offen sc1 lds\n\ts_mov_b32 m0, %0"
               : "=&s"(keep) : "v"(voff), "s"(r), "s"(soff), "s"(lds) : "memory");
}
#define VMWAIT(n) asm volatile("s_waitcnt vmcnt(%0)" :: "n"(n) : "memory")

extern "C"
#if defined(__HIP_DEVICE_COMPILE__)
__attribute__((target("no-packed-fp32-ops")))
#endif
__global__ __launch_bounds__(512, 2) void k3_tail(const u16* __restrict__ x, char* __restrict__ ws, u16* __restrict__ out) {
  __shared__ __attribute__((aligned(16))) char sm[L_END];
  const int tid = threadIdx.x, xcd = blockIdx.x & 7, idx = blockIdx.x >> 3;
  const int s2 = 4 * (xcd >> 1) + (idx & 3), j = 8 * (xcd & 1) + (idx >> 2);
  const int w = __builtin_amdgcn_readfirstlane(tid >> 6), l = tid & 63, ln = l & 15, g = l >> 4;
  const int wq = w & 3, kh = w >> 2, vo = l * 16;
  const rsrc_t rw = mkrs(ws, WS_USED);
  const f32x4 z4 = {0.f, 0.f, 0.f, 0.f};
  STAMP(2, 0);
  u32x4 wreg[30], wx[3];
  const int wb = WP_OFF + ((j * 4 + wq) * 60 + kh * 30) * 1024;
#define W_LOAD(A, B) _Pragma("unroll") for (int i = (A); i < (B); ++i) wreg[i] = ld128(rw, vo, wb + i * 1024)
  const int cg = (j + 4 * (idx & 3)) & 15;
  if (cg < 10) {
    u32x2 fa[4], ny;
    {
      const int so = UP_OFF + ((w & 1) * 16 + s2) * 1280 + cg * 128;
#pragma unroll
      for (int i = 0; i < 4; ++i) fa[i] = ld64(rw, 4 * g * 2 * 16 * 1280 + ln * 8, so + (16 * (w >> 1) + i) * 2 * 16 * 1280);
      ny = ld64(rw, ln * 8, so + 128 * 16 * 1280);
    }
    SB();
    const rsrc_t rx = mkrs(x, 2048 * 640 * 2);
    u32x4 xv[2];
    f32x4 gav[2][2];
#pragma unroll
    for (int hf = 0; hf < 2; ++hf) xv[hf] = ld128(rx, ln * 16 * 1280 + g * 16, (16 * 16 * w + s2) * 1280 + cg * 64 + hf * 640);
#pragma unroll
    for (int hf = 0; hf < 2; ++hf)
#pragma unroll
      for (int m = 0; m < 2; ++m) gav[hf][m] = ldf4(rw, g * 32, MOD_OFF + (1280 + 32 * cg + 320 * hf + 4 * m) * 4);
    SB();
    u32x4 bc[4];
    {
      const int s1 = 16 * w + ln;
      const float step = (float)s1 * (1.f / 128.f), base = step * (float)(4 * g);
      const float t16 = 2.f * __builtin_amdgcn_cosf(step * 16.f);
#pragma unroll
      for (int i = 0; i < 4; ++i) {
        const float a0 = fmaf(step, (float)i, base), a1 = fmaf(step, (float)(16 + i), base);
        const float c0 = __builtin_amdgcn_cosf(a0), s0 = __builtin_amdgcn_sinf(a0), c1 = __builtin_amdgcn_cosf(a1), s1_ = __builtin_amdgcn_sinf(a1);
        const float c2 = fmaf(t16, c1, -c0), s2_ = fmaf(t16, s1_, -s0);
        bc[0][i] = pk2(c0, s0); bc[1][i] = pk2(c1, s1_); bc[2][i] = pk2(c2, s2_); bc[3][i] = pk2(fmaf(t16, c2, -c1), fmaf(t16, s2_, -s1_));
      }
      if (g == 0) bc[0][0] = (s1 & 1) ? 0xbf003f00u : 0x3f003f00u;
      pin(bc[0]); pin(bc[1]); pin(bc[2]); pin(bc[3]);
    }
    SB();
    W_LOAD(0, 3);
    SB();
#pragma unroll
    for (int mu = 0; mu < 2; ++mu) {
      if (w < 2 && g == 0) fa[0][mu] = __builtin_amdgcn_perm(ny[mu], fa[0][mu], 0x05040100u);
      *(u32x4*)(sm + (w * 2 + mu) * 1024 + vo) = u32x4{fa[0][mu], fa[1][mu], fa[2][mu], fa[3][mu]};
    }
    STAMP(2, 10);
    bar();
    STAMP(2, 11);
    W_LOAD(3, 6);
    SB();
    f32x4 acc[2][2];
#pragma unroll
    for (int kk = 0; kk < 4; ++kk)
#pragma unroll
      for (int pp = 0; pp < 2; ++pp)
#pragma unroll
        for (int mu = 0; mu < 2; ++mu)
          acc[pp][mu] = mfma16(*(const u32x4*)(sm + ((kk * 2 + pp) * 2 + mu) * 1024 + vo), bc[kk], kk == 0 ? z4 : acc[pp][mu]);
    SB();
    W_LOAD(6, 9);
    SB();
    float ss = 0.f, qq = 0.f;
#pragma unroll
    for (int hf = 0; hf < 2; ++hf) {
      u32x4 o;
#pragma unroll
      for (int r = 0; r < 4; ++r) {
        const float y0 = hf ? acc[0][0][r] - acc[1][0][r] : acc[0][0][r] + acc[1][0][r];
        const float y1 = hf ? acc[0][1][r] - acc[1][1][r] : acc[0][1][r] + acc[1][1][r];
        o[r] = pk2(fmaf(gav[hf][r >> 1][2 * (r & 1)], y0, lo2f(xv[hf][r])), fmaf(gav[hf][r >> 1][2 * (r & 1) + 1], y1, hi2f(xv[hf][r])));
      }
      st128c(o, rw, vo, X1_OFF + ((s2 * 20 + cg + 10 * hf) * 8 + w) * 1024);
#pragma unroll
      for (int e = 0; e < 4; ++e) { ss = dot2(o[e], 0x3f803f80u, ss); qq = dot2(o[e], o[e], qq); }
    }
    {
      const auto a = __builtin_amdgcn_permlane32_swap(f2u(ss), f2u(qq), false, false);
      const u32 z = f2u(u2f(a[0]) + u2f(a[1]));
      const auto b = __builtin_amdgcn_permlane16_swap(z, z, false, false);
      __builtin_amdgcn_raw_buffer_store_b32(f2u(u2f(b[0]) + u2f(b[1])), rw, (g & 1) ? 0x7ff00000 : ln * 8 + (g >> 1) * 4,
                                            STAT_OFF + ((s2 * 10 + cg) * 128 + 16 * w) * 8, 16);
    }
    STAMP(2, 12);
    SB();
    W_LOAD(9, 14);
    VMWAIT(5);
    bar();
    if (tid == 0) __builtin_amdgcn_raw_buffer_store_b32(1u, rw, 0, MARK_OFF + s2 * 128 + cg * 4, 16);
  } else {
    W_LOAD(0, 14);
  }
  STAMP(2, 1);
  if (w == 7) {
#pragma unroll 1
    for (u32 sp = 0; sp < SPIN_CAP; ++sp) {
      asm volatile("" ::: "memory");
      if (!any_sent(__builtin_amdgcn_raw_buffer_load_b32(rw, ln < 10 ? ln * 4 : 0, MARK_OFF + s2 * 128, 16))) break;
      __builtin_amdgcn_s_sleep(1);
    }
  }
  W_LOAD(14, W_C);
  bar();
  STAMP(2, 6);
  const int sub = (w >> 1) & 1;
  const int xsrc = X1_OFF + s2 * 160 * 1024 + (10 * kh + 5 * (w & 1)) * 8192;
  const int tS = (kh ^ 1) * 1024, tF = kh * 1024;
  const u32 ldst = (u32)(size_t)(LAS char*)sm + L_TOK + 5 * w * 1024;
#define TOK_TILE(b) ((b) == 0 ? tS + sub * 2048 : (b) == 3 ? tF + 4096 + sub * 2048 : sub ? tS + ((b) + 1) * 2048 : tF + ((b) - 1) * 2048)
#define TOK_DMA(b, i) dma1k(rw, vo, xsrc + (i) * 8192 + TOK_TILE(b), ldst + ((b) & 1) * 40960 + (i) * 1024)
  {
    const u32x4 pa = ld128c(rw, (l & 7) * 16 + (l >> 3) * 1024, STAT_OFF + s2 * 10240 + w * 128);
    const u32x4 pb = ld128c(rw, l < 16 ? (l & 7) * 16 + (l >> 3) * 1024 : 0x7ff00000, STAT_OFF + s2 * 10240 + 8192 + w * 128);
    SB();
#pragma unroll
    for (int i = 0; i < 5; ++i) TOK_DMA(0, i);
    W_LOAD(W_C, W_C + 3);
    SB();
    f32x4 p = __builtin_bit_cast(f32x4, pa) + __builtin_bit_cast(f32x4, pb);
#pragma unroll
    for (int e = 0; e < 4; ++e) {
      float v = p[e];
      v += dppf<0x128>(v);
      const auto a = __builtin_amdgcn_permlane16_swap(f2u(v), f2u(v), false, false);
      v = u2f(a[0]) + u2f(a[1]);
      const auto b = __builtin_amdgcn_permlane32_swap(f2u(v), f2u(v), false, false);
      p[e] = u2f(b[0]) + u2f(b[1]);
    }
    float mb, sd;
    ln_scalars((l & 8) ? p[2] : p[0], (l & 8) ? p[3] : p[1], mb, sd);
    const float C0 = -2.f * 0.7978845608028654f * 1.4426950408889634f, C1 = C0 * 0.044715f;
    const float r = __builtin_amdgcn_rcpf(sd);
    const u32 nm = pk2(-mb, -mb), sp = pk2(sd, sd);
    const int tk = 16 * w + 2 * (l & 7) + ((l >> 3) & 1);
    if (g == 0) *(f32x4*)(sm + L_ST + tk * 16) = f32x4{C0 * r, C1 * r * r * r, sd * sd, 0.f};
    if (g == 1) *(u32x4*)(sm + L_MS + tk * 16) = u32x4{nm, nm & 0xffffu, sp, sp & 0xffffu};
  }
  f32x4 A[8][3], cst, acc[2][5];
  u32x4 hq, bq[3], bm, wde[NWE], h[4][6], xr[4];
  f32x4 gbv;
  float z0, z1, p0, p1;
  u32 mk;
  const char* tok = sm + L_TOK + kh * 20480 + vo;
  const int hbase = g < 3 ? HH_OFF + (s2 * 48 + 3 * j) * 8192 + kh * 1024 + (16 * wq + ln) * 16 + g * 8192 : 0x7ff00000;
#define PIN1(x) asm volatile("" : "+v"(x))
#define EPI_A(K) do { PIN1(K); z0 = K[0] * fmaf(K[0] * K[0], cst[1], cst[0]); z1 = K[1] * fmaf(K[1] * K[1], cst[1], cst[0]); PIN1(z0); PIN1(z1); } while (0)
#define EPI_B(K) do { PIN1(z0); p0 = K[0] * K[2]; p1 = K[1] * K[3]; \
    z0 = __builtin_amdgcn_exp2f(z0); z1 = __builtin_amdgcn_exp2f(z1); PIN1(z0); PIN1(z1); PIN1(p0); PIN1(p1); } while (0)
#define EPI_C() do { PIN1(z0); z0 = __builtin_amdgcn_rcpf(fmaf(z0, cst[2], cst[2])); z1 = __builtin_amdgcn_rcpf(fmaf(z1, cst[2], cst[2])); PIN1(z0); PIN1(z1); } while (0)
#define EPI_D(t) do { PIN1(z0); u32 h_ = umin(pk2(p0 * z0, p1 * z1), SENT - 1u); PIN1(h_); hq[t] = h_; } while (0)
#define EPI_STORE(P) do {   \
    const auto a_ = __builtin_amdgcn_permlane32_swap(hq[0], hq[2], false, false); \
    const auto b_ = __builtin_amdgcn_permlane32_swap(hq[1], hq[1], false, false); \
    const auto c_ = __builtin_amdgcn_permlane16_swap(a_[0], b_[0], false, false); \
    const auto d_ = __builtin_amdgcn_permlane16_swap(a_[1], b_[1], false, false); \
    st128c(u32x4{c_[0], c_[1], d_[0], d_[1]}, rw, hbase, (P) * 2048); } while (0)
#define EPI_CST(P) cst = *(const f32x4*)(sm + L_ST + (32 * (P) + 16 * kh + ln) * 16)
#define EPI_STEP(e, PH, P) do { if ((e) >= 0 && (e) < 12) { if (((e) & 3) == 0) EPI_A(A[PH][((e) >> 2) % 3]); if (((e) & 3) == 1) EPI_B(A[PH][((e) >> 2) % 3]); \
      if (((e) & 3) == 2) EPI_C(); if (((e) & 3) == 3) EPI_D(((e) >> 2) % 3); } if ((e) == 12) EPI_STORE(P); } while (0)
#define XB_WRITE(PH, T, t) *(f32x4*)(sm + L_XB + ((T) & 1) * 24576 + (w * 3 + (t)) * 1024 + vo) = A[PH][t]
#define XB_READ(PH, T) _Pragma("unroll") for (int t = 0; t < 3; ++t) A[PH][t] = *(const f32x4*)(sm + L_XB + ((T) & 1) * 24576 + ((w ^ 4) * 3 + t) * 1024 + vo)
#define SGB(mask, n) __builtin_amdgcn_sched_group_barrier(mask, n, 0)
#define FRAG(n) (*(const u32x4*)(tok + (((n) / 20) & 1) * 40960 + (((n) / 10) & 1) * 10240 + ((n) % 10) * 1024))
  const int cgrp = 4 * (xcd & 1) + ((idx >> 2) & 3), hf2 = idx >> 4;
  const int db = WD_OFF + (cgrp * 48 + 6 * w) * 5 * 1024;
  const int hb = HH_OFF + ((s2 * 48 + 6 * w) * 8 + hf2) * 1024;
#define HH_LD(T, i) ld128c(rw, vo, hb + (i) * 8192 + (T) * 2048)
  VMWAIT(3);
  bar();
  STAMP(2, 3);
  bq[0] = FRAG(0);
  bq[1] = FRAG(1);
  auto g1_step = [&](auto nc) __attribute__((always_inline)) {
    constexpr int n = decltype(nc)::value;
    constexpr int ph = n / 10, kk = n % 10;
    constexpr bool isS = ph == 0 || ph == 1 || ph == 3 || ph == 5;
    if (n == 59 - 2 && w == 7) {
#pragma unroll 1
      for (u32 sp = 0; any_sent(mk) && sp < SPIN_CAP; ++sp) {
        __builtin_amdgcn_s_sleep(1);
        asm volatile("" ::: "memory");
        mk = __builtin_amdgcn_raw_buffer_load_b32(rw, ln * 4, MARK_OFF + s2 * 128 + 64, 16);
      }
    }
    if (n == 18) VMWAIT(3);
    if (n == 38) VMWAIT(NWE);
    if (n == 58) VMWAIT(2);
    if (n == 18 || n == 38 || n == 58 || n == 68) bar();
    if (n == 38 && tid == 0) __builtin_amdgcn_raw_buffer_store_b32(1u, rw, 0, MARK_OFF + s2 * 128 + 64 + j * 4, 16);
    if (n == 20) STAMP(2, 14);
    if (n == 60) STAMP(2, 13);
    if constexpr (n + 2 < 80) bq[(n + 2) % 3] = FRAG(n + 2);
    if (isS && kk == 7) bm = *(const u32x4*)(sm + L_MS + (32 * ((ph + 1) >> 1) + 16 * (kh ^ 1) + ln) * 16);
    if (n == 18) { XB_READ(2, 0); }
    if (n == 38) { XB_READ(4, 1); }
    if (n == 58) { XB_READ(6, 2); }
    if (n == 68) { XB_READ(7, 3); }
#pragma unroll
    for (int t = 0; t < 3; ++t) A[ph][t] = mfma16(wreg[kk * 3 + t], bq[n % 3], (kk == 0 && isS) ? z4 : A[ph][t]);
    if (isS && kk == 9) {
#pragma unroll
      for (int t = 0; t < 3; ++t) A[ph][t] = mfma16(wx[t], bm, A[ph][t]);
    }
    if constexpr (n >= 11 && n < 14) XB_WRITE(0, 0, n - 11);
    if constexpr (n >= 21 && n < 24) XB_WRITE(1, 1, n - 21);
    if constexpr (n >= 41 && n < 44) XB_WRITE(3, 2, n - 41);
    if constexpr (n >= 61 && n < 64) XB_WRITE(5, 3, n - 61);
    if constexpr (n < (27 - W_C) / 3) { W_LOAD(W_C + 3 + 3 * n, W_C + 6 + 3 * n); }
    if constexpr (n == (27 - W_C) / 3) {
#pragma unroll
      for (int t = 0; t < 3; ++t) wx[t] = ld128(rw, g == 0 ? vo : 0x7ff00000, CS_OFF + ((j * 4 + wq) * 3 + t) * 256);
    }
    if constexpr (n >= 4 && n < 9) TOK_DMA(1, n - 4);
    if constexpr (n >= 10 && n < 13) st128c(u32x4{SENT, SENT, SENT, SENT}, rw, vo, HH_OFF + (s2 * 48 + 3 * j + n - 10) * 8192 + w * 1024);
    if constexpr (n >= 19 && n < 24) TOK_DMA(2, n - 19);
    if constexpr (n >= 24 && n < 24 + NWE) wde[n - 24] = ld128(rw, vo, db + (n - 24) * 1024);
    if constexpr (n >= 39 && n < 44) TOK_DMA(3, n - 39);
    if (n == 30) EPI_CST(0);
    if (n == 50) EPI_CST(1);
    if (n == 69) EPI_CST(2);
    if constexpr (n >= 32 && n < 44) EPI_STEP(n - 32, 2, 0);
    if constexpr (n == 45) EPI_STORE(0);
    if constexpr (n >= 52 && n < 65) EPI_STEP(n - 52, 4, 1);
    if constexpr (n >= 71 && n < 77) { EPI_STEP(2 * (n - 71), 6, 2); EPI_STEP(2 * (n - 71) + 1, 6, 2); }
    if constexpr (n == 77) EPI_STORE(2);
    if (n == 50) mk = __builtin_amdgcn_raw_buffer_load_b32(rw, ln * 4, MARK_OFF + s2 * 128 + 64, 16);
    if constexpr (n >= 60 && n < 63) wx[n - 60] = ld128(rw, vo, db + (NWE + n - 60) * 1024);
    if constexpr (n >= 65 && n < 71) h[0][n - 65] = HH_LD(0, n - 65);
    if constexpr (n >= 71 && n != 77) wreg[n - 71 - (n > 77)] = ld128(rw, vo, db + (NWE + 3 + n - 71 - (n > 77)) * 1024);
#pragma unroll
    for (int i = 0; i < 3; ++i) { SGB(0x8, 1); SGB(0x100, 1); SGB(0x402, n >= 70 ? 4 : 2); SGB(0x220, 1); }
    SB();
  };
  static_for<0, 80>(g1_step);
  STAMP(2, 4);
  const int rsel = w < 4 ? w : 4;
  const int rc0 = 80 * cgrp + 16 * rsel + 4 * g;
  const rsrc_t ro = mkrs(out, 2048 * 640 * 2);
#define WD(i) ((i) < NWE ? wde[(i) < NWE ? (i) : 0] : (i) < NWE + 3 ? wx[(i) >= NWE && (i) < NWE + 3 ? (i) - NWE : 0] : wreg[(i) < NWE + 3 ? 0 : (i) - NWE - 3])
#define G2_REDUCE(T) do { if (w < 4 || w == 4 + (T)) { \
    f32x4 s_ = *(const f32x4*)(sm + ((T) & 1) * 40960 + rsel * 1024 + vo); \
    _Pragma("unroll") for (int sw = 1; sw < 8; ++sw) s_ += *(const f32x4*)(sm + ((T) & 1) * 40960 + (sw * 5 + rsel) * 1024 + vo); \
    const u32 x0_ = (g & 1) ? xr[T][2] : xr[T][0], x1_ = (g & 1) ? xr[T][3] : xr[T][1]; \
    st64(u32x2{pk2(fmaf(gbv[0], s_[0], lo2f(x0_)), fmaf(gbv[1], s_[1], hi2f(x0_))), pk2(fmaf(gbv[2], s_[2], lo2f(x1_)), fmaf(gbv[3], s_[3], hi2f(x1_)))}, \
         ro, ln * 16 * 1280 + rc0 * 2, (16 * 16 * (hf2 + 2 * (T)) + s2) * 1280); } } while (0)
#define HH_CHECK(H, T) do { u32 m_ = 0u; _Pragma("unroll") for (int i = 0; i < 6; ++i) m_ = mx4(m_, H[i]); \
    if (__builtin_expect(any_sent(m_), 0)) { u32 sp_ = 0; _Pragma("unroll 1") do { __builtin_amdgcn_s_sleep(2); asm volatile("" ::: "memory"); \
      _Pragma("unroll") for (int i = 0; i < 6; ++i) H[i] = HH_LD(T, i); \
      m_ = 0u; _Pragma("unroll") for (int i = 0; i < 6; ++i) m_ = mx4(m_, H[i]); } while (any_sent(m_) && ++sp_ < SPIN_CAP); } } while (0)
#define G2_PART(T, t) *(f32x4*)(sm + ((T) & 1) * 40960 + (w * 5 + (t)) * 1024 + vo) = acc[(T) & 1][t]
  EPI_CST(3);
  constexpr int WR0 = 8, WRN = 27 - NWE;
  auto g2_step = [&](auto nc) __attribute__((always_inline)) {
    constexpr int n = decltype(nc)::value;
    constexpr int T = n / 6, kk = n % 6;
    if (kk == 0) HH_CHECK(h[T], T);
    if (T > 0 && kk == 4) bar();
#pragma unroll
    for (int t = 0; t < 5; ++t) acc[T & 1][t] = mfma16(WD(kk * 5 + t), h[T][kk], kk == 0 ? z4 : acc[T & 1][t]);
    if constexpr (n < 3) {
      EPI_STEP(4 * n, 7, 3); EPI_STEP(4 * n + 1, 7, 3); EPI_STEP(4 * n + 2, 7, 3); EPI_STEP(4 * n + 3, 7, 3);
#pragma unroll
      for (int i = WR0 + 2 * n; i < WR0 + 2 * n + 2; ++i) wreg[i] = ld128(rw, vo, db + (NWE + 3 + i) * 1024);
      if (n == 2) {
        EPI_STORE(3);
#pragma unroll
        for (int i = WR0 + 6; i < WRN; ++i) wreg[i] = ld128(rw, vo, db + (NWE + 3 + i) * 1024);
      }
    }
    if constexpr (n >= 3 && n < 6) { h[1][2 * (n - 3)] = HH_LD(1, 2 * (n - 3)); h[1][2 * (n - 3) + 1] = HH_LD(1, 2 * (n - 3) + 1); }
    if constexpr (n == 5) {
      gbv = ldf4(rw, rc0 * 4, MOD_OFF + 3200 * 4);
#pragma unroll
      for (int q = 0; q < 4; ++q) xr[q] = ld128c(rw, (rc0 >> 5) * 8192 + (((rc0 >> 3) & 3) * 16 + ln) * 16, X1_OFF + (s2 * 160 + hf2 + 2 * q) * 1024);
#pragma unroll
      for (int i = 0; i < H2E; ++i) h[2][i] = HH_LD(2, i);
    }
    if constexpr (n >= 6 && n < 12 - H2E) h[2][n - 6 + H2E] = HH_LD(2, n - 6 + H2E);
    if constexpr (n >= 12 - H2E && n < 18 - H2E) h[3][n - 12 + H2E] = HH_LD(3, n - 12 + H2E);
    if constexpr (T > 0 && kk < 3) {
      G2_PART(T - 1, 2 * kk);
      if constexpr (kk < 2) G2_PART(T - 1, 2 * kk + 1);
    }
    if constexpr (T > 0 && kk == 4) G2_REDUCE(T - 1);
#pragma unroll
    for (int i = 0; i < 5; ++i) { SGB(0x8, 1); SGB(0x402, n < 3 ? 5 : 2); SGB(0x320, 1); }
    if (n == 5) STAMP(2, 7);
    if (n == 11) STAMP(2, 15);
    if (n == 17) STAMP(2, 5);
    SB();
  };
  static_for<0, 24>(g2_step);
  STAMP(2, 8);
#pragma unroll
  for (int t = 0; t < 5; ++t) G2_PART(3, t);
  bar();
  G2_REDUCE(3);
  STAMP(2, 9);
}

extern "C" __global__ void k9_end() {}

extern "C" void kernel_launch(void* const* d_in, const int* in_sizes, int n_in, void* d_out, int out_size, void* d_ws,
                              size_t ws_size, hipStream_t stream) {
  const u16* x = (const u16*)d_in[0];
  const u16* wr = (const u16*)d_in[1];
  const u16* wi = (const u16*)d_in[2];
  const u16* wg = (const u16*)d_in[4];
  const u16* wu = (const u16*)d_in[5];
  const u16* wd = (const u16*)d_in[6];
  const u16* c = (const u16*)d_in[7];
  const u16* wmod = (const u16*)d_in[8];
  char* ws = (char*)d_ws;
  k1_ln_dft128<<<256, 512, 0, stream>>>(x, c, wmod, wr, wi, ws);
  k2_spec<<<256, 512, 0, stream>>>(wg, wu, wd, ws);
  k3_tail<<<256, 512, 0, stream>>>(x, ws, (u16*)d_out);
}
```

```cpp
#include <hip/hip_runtime.h>

typedef unsigned short u16;
typedef unsigned int u32;
typedef __bf16 bf16x8 __attribute__((ext_vector_type(8)));
typedef __bf16 bf16x2 __attribute__((ext_vector_type(2)));
typedef float f32x2 __attribute__((ext_vector_type(2)));
typedef float f32x4 __attribute__((ext_vector_type(4)));
typedef float f32x16 __attribute__((ext_vector_type(16)));
typedef u32 u32x2 __attribute__((ext_vector_type(2)));
typedef u32 u32x4 __attribute__((ext_vector_type(4)));
typedef short s16x4 __attribute__((ext_vector_type(4)));
typedef __amdgpu_buffer_rsrc_t rsrc_t;
#define LAS __attribute__((address_space(3)))
#define DI __device__ __forceinline__
#define SB() __builtin_amdgcn_sched_barrier(0)

constexpr int D_ = 640, FF_ = 1536;
constexpr int MOD_OFF = 0;
constexpr int CS_OFF = 16384;
constexpr int STAT_OFF = 65536;
constexpr int MARK_OFF = STAT_OFF + 163840;
constexpr int U_OFF = 262144;
constexpr int UP_OFF = U_OFF + 2621440;
constexpr int X1_OFF = UP_OFF + 2662400;
constexpr int WP_OFF = X1_OFF + 2621440;
constexpr int WD_OFF = WP_OFF + 3932160;
constexpr int HH_OFF = WD_OFF + 1966080;
constexpr int FP_OFF = HH_OFF + 6291456;
constexpr int WS_USED = FP_OFF + 2662400;
constexpr u32 SENT = 0xffffffffu;

DI u32 pk2(float a, float b) {
  f32x2 v = {a, b};
  return __builtin_bit_cast(u32, __builtin_convertvector(v, bf16x2));
}
DI u32 pk2v(f32x2 v) { return __builtin_bit_cast(u32, __builtin_convertvector(v, bf16x2)); }
DI float rbf(float a) { return __builtin_bit_cast(float, pk2(a, 0.f) << 16); }
DI float u2f(u32 v) { return __builtin_bit_cast(float, v); }
DI u32 f2u(float v) { return __builtin_bit_cast(u32, v); }
DI float lo2f(u32 v) { return __builtin_bit_cast(float, v << 16); }
DI float hi2f(u32 v) { return __builtin_bit_cast(float, v & 0xffff0000u); }
DI f32x2 lo2(const f32x4& v) { return f32x2{v[0], v[1]}; }
DI f32x2 hi2(const f32x4& v) { return f32x2{v[2], v[3]}; }
DI f32x2 fma2(f32x2 a, f32x2 b, f32x2 c) { return __builtin_elementwise_fma(a, b, c); }
DI f32x4 mfma16(u32x4 a, u32x4 b, f32x4 c) {
  return __builtin_amdgcn_mfma_f32_16x16x32_bf16(__builtin_bit_cast(bf16x8, a), __builtin_bit_cast(bf16x8, b), c, 0, 0, 0);
}
DI f32x16 mfma32(u32x4 a, u32x4 b, f32x16 c) {
  return __builtin_amdgcn_mfma_f32_32x32x16_bf16(__builtin_bit_cast(bf16x8, a), __builtin_bit_cast(bf16x8, b), c, 0, 0, 0);
}
DI u32x2 trread(const char* p) {
  s16x4 r = __builtin_amdgcn_ds_read_tr16_b64_v4i16((LAS s16x4*)(p));
  return __builtin_bit_cast(u32x2, r);
}
DI u32x4 trread2(const char* p, int d) {
  const u32x2 a = trread(p), b = trread(p + d);
  return u32x4{a[0], a[1], b[0], b[1]};
}
DI float cosr(float rev) { return __builtin_amdgcn_cosf(__builtin_amdgcn_fractf(rev)); }
DI float dot2(u32 a, u32 b, float c) {
  return __builtin_amdgcn_fdot2_f32_bf16(__builtin_bit_cast(bf16x2, a), __builtin_bit_cast(bf16x2, b), c, false);
}
template <int CTRL> DI float dppf(float v) {
  return __builtin_bit_cast(float, __builtin_amdgcn_update_dpp(0, __builtin_bit_cast(int, v), CTRL, 0xf, 0xf, true));
}
DI rsrc_t mkrs(const void* p, int bytes) { return __builtin_amdgcn_make_buffer_rsrc((void*)p, 0, bytes, 0x00020000); }
DI u32x4 ld128(rsrc_t r, int v, int s) { return __builtin_amdgcn_raw_buffer_load_b128(r, v, s, 0); }
DI u32x2 ld64(rsrc_t r, int v, int s) { return __builtin_amdgcn_raw_buffer_load_b64(r, v, s, 0); }
DI u32 ld32(rsrc_t r, int v, int s) { return __builtin_amdgcn_raw_buffer_load_b32(r, v, s, 0); }
DI f32x4 ldf4(rsrc_t r, int v, int s) { return __builtin_bit_cast(f32x4, ld128(r, v, s)); }
DI void st128(u32x4 d, rsrc_t r, int v, int s) { __builtin_amdgcn_raw_buffer_store_b128(d, r, v + s, 0, 0); }
DI u32x4 ld128c(rsrc_t r, int v, int s) { return __builtin_amdgcn_raw_buffer_load_b128(r, v, s, 16); }
DI void st128c(u32x4 d, rsrc_t r, int v, int s) { __builtin_amdgcn_raw_buffer_store_b128(d, r, v + s, 0, 16); }
DI void st64c(u32x2 d, rsrc_t r, int v, int s) { __builtin_amdgcn_raw_buffer_store_b64(d, r, v, s, 16); }
DI void st64(u32x2 d, rsrc_t r, int v, int s) { __builtin_amdgcn_raw_buffer_store_b64(d, r, v, s, 0); }
DI void st32(u32 d, rsrc_t r, int v, int s) { __builtin_amdgcn_raw_buffer_store_b32(d, r, v, s, 0); }
DI void pin(u32x4& v) { asm volatile("" : "+v"(v)); }
DI void bar() { asm volatile("s_waitcnt lgkmcnt(0)" ::: "memory"); __builtin_amdgcn_s_barrier(); asm volatile("" ::: "memory"); }
DI void ln_scalars(float s, float q, float& mb, float& sd) {
  const float m = s * (1.f / 640.f);
  const float var = fmaxf(fmaf(-m, m, q * (1.f / 640.f)), 0.f);
  mb = rbf(m);
  sd = rbf(__builtin_amdgcn_sqrtf(rbf(rbf(var) + 1e-5f)));
}

#define STAMP(K, I)
#define STAMPW(K, I, W)

DI u32x4 touch_code(int lane) {
  unsigned long long pc;
  int o1, o2;
  asm volatile("s_getpc_b64 %0\n1:\n\ts_mov_b32 %1, k2_spec-1b\n\ts_mov_b32 %2, k9_end-1b" : "=s"(pc), "=s"(o1), "=s"(o2));
  const rsrc_t rc = mkrs((const void*)(pc + (long long)o1), o2 - o1);
  return u32x4{ld32(rc, lane * 128, 0), ld32(rc, lane * 128 + 8192, 0), ld32(rc, lane * 128 + 16384, 0), ld32(rc, lane * 128 + 24576, 0)};
}

extern "C" __global__ __launch_bounds__(512, 2) void k1_ln_dft128(const u16* __restrict__ x, const u16* __restrict__ cvec,
                                                                   const u16* __restrict__ wmod, const u16* __restrict__ wr_,
                                                                   const u16* __restrict__ wi_, char* __restrict__ ws) {
  __shared__ __attribute__((aligned(16))) char sm[13 * 2688 + 64];
  const int tid = threadIdx.x, xcd = blockIdx.x & 7, idx = blockIdx.x >> 3;
  const int w = __builtin_amdgcn_readfirstlane(tid >> 6), l = tid & 63;
  const rsrc_t rw = mkrs(ws, WS_USED);
  STAMP(0, 0);
  if (__builtin_expect(idx < 10, 1)) {
    const int s2 = 2 * xcd + idx / 5, cg = idx % 5;
    const int j8 = tid & 7, rr = tid >> 3;
    const rsrc_t rx = mkrs(x, 2048 * 640 * 2);
    u32x4 v[2][10];
    const int vo = (16 * rr + s2) * 1280 + j8 * 16;
#define XCH(i) ((2 * cg + (i) >= 10 ? 2 * cg + (i) - 10 : 2 * cg + (i)) * 128)
#pragma unroll
    for (int i = 0; i < 10; ++i) v[0][i] = ld128(rx, vo, XCH(i));
    SB();
    const int h = l >> 5, Mt = w & 3, nh = w >> 2;
    u32x4 ac[8];
    {
      const int row = l & 31, j = 16 * Mt + (row >> 1), part = row & 1;
      const int jj = (part && j == 0) ? 64 : j;
      const float step = (float)jj * (1.f / 128.f);
      const float a0 = fmaf(step, (float)(8 * h), (part && j != 0) ? 0.25f : 0.f);
      f32x2 c0[4], c1[4];
#pragma unroll
      for (int e = 0; e < 8; ++e) {
        c0[e >> 1][e & 1] = __builtin_amdgcn_cosf(fmaf(step, (float)e, a0));
        c1[e >> 1][e & 1] = __builtin_amdgcn_cosf(fmaf(step, (float)(16 + e), a0));
      }
      const float t16 = 2.f * __builtin_amdgcn_cosf(step * 16.f);
#pragma unroll
      for (int kap = 0; kap < 8; ++kap) {
#pragma unroll
        for (int e = 0; e < 4; ++e) ac[kap][e] = pk2v(c0[e]);
        pin(ac[kap]);
        if (kap < 7) {
#pragma unroll
          for (int e = 0; e < 4; ++e) { const f32x2 n = fma2(f32x2{t16, t16}, c1[e], -c0[e]); c0[e] = c1[e]; c1[e] = n; }
        }
      }
    }
    SB();
    STAMP(0, 1);
    f32x16 acc[2];
#pragma unroll
    for (int i = 0; i < 16; ++i) { acc[0][i] = 0.f; acc[1][i] = 0.f; }
    const int blk = (l >> 4) & 1, q4 = (l & 15) >> 2, p4 = l & 3;
    const char* rb0 = sm + (8 * h + q4) * 256 + (((2 * nh) ^ q4) << 6) + 32 * blk + 8 * p4;
    const char* rb1 = sm + (8 * h + q4) * 256 + (((2 * nh + 1) ^ q4) << 6) + 32 * blk + 8 * p4;
    char* wb0 = sm + rr * 256 + ((((j8 >> 2)) ^ (rr & 3)) << 6) + (j8 & 3) * 16;
    char* wb1 = sm + rr * 256 + (((2 + (j8 >> 2)) ^ (rr & 3)) << 6) + (j8 & 3) * 16;
#pragma unroll
    for (int ps = 0; ps < 2; ++ps) {
      float s = 0.f, q = 0.f, s_ = 0.f, q_ = 0.f;
#pragma unroll
      for (int i = 0; i < 10; ++i) {
        if (ps == 0) v[1][i] = ld128(rx, vo, 64 * 16 * 1280 + XCH(i));
        s = dot2(v[ps][i][0], 0x3f803f80u, s); q = dot2(v[ps][i][0], v[ps][i][0], q);
        s_ = dot2(v[ps][i][1], 0x3f803f80u, s_); q_ = dot2(v[ps][i][1], v[ps][i][1], q_);
        s = dot2(v[ps][i][2], 0x3f803f80u, s); q = dot2(v[ps][i][2], v[ps][i][2], q);
        s_ = dot2(v[ps][i][3], 0x3f803f80u, s_); q_ = dot2(v[ps][i][3], v[ps][i][3], q_);
        SB();
      }
      s += s_; q += q_;
      s += dppf<0xB1>(s); q += dppf<0xB1>(q);
      s += dppf<0x4E>(s); q += dppf<0x4E>(q);
      s += dppf<0x141>(s); q += dppf<0x141>(q);
      float mb, sd;
      ln_scalars(s, q, mb, sd);
      const float r = __builtin_amdgcn_rcpf(sd), mr = -mb * r;
#pragma unroll
      for (int c = 0; c < 2; ++c) {
        u32x4 o;
#pragma unroll
        for (int e = 0; e < 4; ++e) o[e] = pk2v(fma2(f32x2{lo2f(v[ps][c][e]), hi2f(v[ps][c][e])}, f32x2{r, r}, f32x2{mr, mr}));
        *(u32x4*)((c ? wb1 : wb0) + ps * 16384) = o;
      }
      bar();
#pragma unroll
      for (int kk = 0; kk < 4; ++kk) {
        acc[0] = mfma32(ac[4 * ps + kk], trread2(rb0 + (4 * ps + kk) * 4096, 1024), acc[0]);
        acc[1] = mfma32(ac[4 * ps + kk], trread2(rb1 + (4 * ps + kk) * 4096, 1024), acc[1]);
      }
      SB();
      if (ps == 0) STAMP(0, 2);
    }
    STAMP(0, 4);
    const int so = ((16 * Mt + 2 * h) * 16 + s2) * 2560 + (128 * cg + 64 * nh + (l & 31)) * 4;
#pragma unroll
    for (int nt = 0; nt < 2; ++nt)
#pragma unroll
      for (int i = 0; i < 16; i += 2) st32(pk2(acc[nt][i], acc[nt][i + 1]), rw, so, U_OFF + (((i & 3) >> 1) + 4 * (i >> 2)) * 16 * 2560 + nt * 128);
  } else if (idx < 22) {
    const int id = xcd * 12 + (idx - 10);
    const rsrc_t rm = mkrs(wmod, 640 * 3840 * 2), rc = mkrs(cvec, 640 * 2);
    const int rs = tid / 5, j5 = tid - rs * 5;
    float* red = (float*)sm;
    u32x4 tc = {0u, 0u, 0u, 0u};
    if (w < 5) {
      u32x4 wv[10];
      u32 cv[10];
#pragma unroll
      for (int i = 0; i < 10; ++i) cv[i] = __builtin_amdgcn_raw_buffer_load_b16(rc, rs * 2, i * 128, 0);
#pragma unroll
      for (int i = 0; i < 10; ++i) wv[i] = ld128(rm, rs * 7680 + id * 80 + j5 * 16, i * 64 * 7680);
      SB();
#pragma unroll
      for (int i = 0; i < 10; ++i) {
        const float c = lo2f(cv[i]);
        cv[i] = pk2(c * __builtin_amdgcn_rcpf(1.f + __expf(-c)), 0.f);
      }
      float a[8];
#pragma unroll
      for (int e = 0; e < 8; ++e) a[e] = 0.f;
#pragma unroll
      for (int i = 0; i < 10; ++i) {
        const u32 shi = cv[i] << 16;
#pragma unroll
        for (int e = 0; e < 4; ++e) { a[2 * e] = dot2(wv[i][e], cv[i], a[2 * e]); a[2 * e + 1] = dot2(wv[i][e], shi, a[2 * e + 1]); }
      }
      *(f32x4*)(red + rs * 40 + j5 * 8) = f32x4{a[0], a[1], a[2], a[3]};
      *(f32x4*)(red + rs * 40 + j5 * 8 + 4) = f32x4{a[4], a[5], a[6], a[7]};
    } else if (w == 7 && idx == 10) {
      tc = touch_code(l);
    }
    bar();
    if (tid < 320) {
      const int col = tid % 40, part = tid / 40;
      float s = 0.f;
#pragma unroll
      for (int i = 0; i < 8; ++i) s += red[(part * 8 + i) * 40 + col];
      red[2560 + part * 40 + col] = s;
    }
    bar();
    if (tid < 40) {
      float s = 0.f;
#pragma unroll
      for (int i = 0; i < 8; ++i) s += red[2560 + i * 40 + tid];
      s = rbf(s);
      const int col = 40 * id + tid, ch = col / 640;
      if (ch == 1 || ch == 4) s = rbf(1.f + s);
      if (ch == 2) s += s;
      ((float*)(ws + MOD_OFF))[col] = s;
    }
    pin(tc);
  } else {
    const int id = xcd * 10 + (idx - 22);
    const rsrc_t rr_ = mkrs(wr_, 2048 * 321 * 2), ri_ = mkrs(wi_, 2048 * 321 * 2);
    u32x4 fch[6];
    int fdst[3];
#pragma unroll
    for (int i = 0; i < 3; ++i) {
      const int n = tid + 512 * i, pc = n / 42, ch = n - pc * 42;
      const int unit = id * 13 + (pc >> 1), ks = (unit >> 4) + 128 * (unit & 15), row = (pc & 1) ? ((2048 - ks) & 2047) : ks;
      const int off = n < 1092 ? ((row * 642) & ~15) + ch * 16 : 0x7ffffff0;
      fch[2 * i] = ld128(rr_, off, 0);
      fch[2 * i + 1] = ld128(ri_, off, 0);
      fdst[i] = pc * 1344 + ch * 16;
    }
    SB();
#pragma unroll
    for (int i = 0; i < 3; ++i)
      if (i < 2 || tid < 68) { *(u32x4*)(sm + fdst[i]) = fch[2 * i]; *(u32x4*)(sm + fdst[i] + 672) = fch[2 * i + 1]; }
    bar();
#pragma unroll 1
    for (int q = w; q < 39; q += 8) {
      const int ul = q / 3, kind = q - 3 * ul, unit = id * 13 + ul;
      const int k1 = unit >> 4, rho = unit & 15, ks = k1 + 128 * rho;
      const int ln = l & 15, p = kind < 2 ? kind : ((l >> 4) & 1), g = kind < 2 ? l >> 4 : 4;
      const int kd0 = 2 * ln + p + 128 * g;
      const char* bd = sm + ul * 2688 + 2 * (ks & 7) + 2 * kd0;
      const char* bm = sm + ul * 2688 + 1344 + 2 * ((-ks) & 7) + 1280 - 2 * kd0;
      u32x4 o;
#pragma unroll
      for (int r = 0; r < 4; ++r) {
        const bool dir = kd0 + 32 * r <= 320;
        const char* src = dir ? bd + 64 * r : bm - 64 * r;
        const u32 a = *(const u16*)src, b = *(const u16*)(src + 672);
        o[r] = (a | (b << 16)) ^ (dir ? 0u : 0x80000000u);
      }
      if (kind == 0 && (l & 31) == 0) {
        const char* src = sm + ul * 2688 + 1344 + 2 * ((-ks) & 7) + (l ? 640 : 0);
        const u32 a2 = *(const u16*)src, b2 = *(const u16*)(src + 672), cur = l ? o[2] : o[0];
        const u32 sym = pk2(0.5f * (lo2f(cur) + lo2f(a2)), 0.5f * (hi2f(cur) - lo2f(b2)));
        if (l) o[2] = sym; else o[0] = sym;
      }
      const int dst = ((k1 * 2 + p) * 8 + (rho >> 1)) * 2560 + (kind < 2 ? (rho & 1) * 1024 + l * 16 : 2048 + ((rho & 1) * 16 + ln) * 16);
      if (kind < 2 || l < 32) st128(o, rw, dst, FP_OFF);
    }
  }
  STAMP(0, 5);
}

constexpr int VP = 1320;
constexpr int CT_OFF = 21504;
enum { SL_D1 = 0, SL_MM = 2, SL_ME = 4, SL_EM = 8, SL_EE = 12, SL_D1I = 14, SL_CI = 16, SL_TFM = 18, SL_TFE = 20, SL_TIM = 22, SL_TIE = 24 };

DI u32x4 twpack(const f32x4& re, const f32x4& im, const f32x4& c, const f32x4& s) {
  const f32x2 a0 = fma2(lo2(im), lo2(s), lo2(re) * lo2(c)), a1 = fma2(hi2(im), hi2(s), hi2(re) * hi2(c));
  const f32x2 b0 = fma2(-lo2(re), lo2(s), lo2(im) * lo2(c)), b1 = fma2(-hi2(re), hi2(s), hi2(im) * hi2(c));
  return u32x4{pk2v(a0), pk2v(a1), pk2v(b0), pk2v(b1)};
}
DI u32x4 fltpack(const f32x4& re, const f32x4& im, const f32x4& c, const f32x4& s) {
  const f32x2 a0 = fma2(-lo2(im), lo2(s), lo2(re) * lo2(c)), a1 = fma2(-hi2(im), hi2(s), hi2(re) * hi2(c));
  const f32x2 b0 = fma2(-lo2(re), lo2(s), -lo2(im) * lo2(c)), b1 = fma2(-hi2(re), hi2(s), -hi2(im) * hi2(c));
  return u32x4{pk2v(a0), pk2v(a1), pk2v(b0), pk2v(b1)};
}

extern "C" __global__ __launch_bounds__(512, 2) void k2_spec(const u16* __restrict__ wg, const u16* __restrict__ wu,
                                                  const u16* __restrict__ wd, char* __restrict__ ws) {
  __shared__ __attribute__((aligned(16))) char sm[CT_OFF + 26 * 1024];
  const int tid = threadIdx.x, xcd = blockIdx.x & 7, idx = blockIdx.x >> 3;
  const rsrc_t rw = mkrs(ws, WS_USED);
  STAMP(1, 0);
  if (idx < 16 || (idx < 18 && xcd == 0)) {
    const int k1 = xcd + 8 * (idx >> 1), p = idx & 1;
    const int w = __builtin_amdgcn_readfirstlane(tid >> 6), l = tid & 63, ln = l & 15, g = l >> 4;
    u32x4 ua[3][2];
    f32x4 cav[3][2], sav[3][2];
    u32x4 fw[3];
    {
      const int ub = U_OFF + (k1 & 63) * 16 * 2560 + w * 64;
      const int vo = 4 * g * 2560 + ln * 4;
      const int u2 = w < 4 ? 1024 : 0;
#pragma unroll
      for (int t = 0; t < 3; ++t)
#pragma unroll
        for (int hf = 0; hf < 2; ++hf)
#pragma unroll
          for (int i = 0; i < 4; ++i) ua[t][hf][i] = ld32(rw, vo, ub + (t < 2 ? t * 512 : u2) + hf * 1280 + i * 2560);
#pragma unroll
      for (int t = 0; t < 3; ++t)
#pragma unroll
        for (int hf = 0; hf < 2; ++hf) cav[t][hf] = ldf4(rw, g * 16, MOD_OFF + 2560 + w * 64 + (t < 2 ? t * 512 : u2) + hf * 1280);
      const int fb = FP_OFF + ((k1 * 2 + p) * 8 + w) * 2560;
      fw[0] = ld128(rw, l * 16, fb);
      fw[1] = ld128(rw, l * 16, fb + 1024);
      fw[2] = ld128(rw, (l & 31) * 16, fb + 2048);
      if (k1 == 0) {
#pragma unroll
        for (int t = 0; t < 3; ++t)
#pragma unroll
          for (int hf = 0; hf < 2; ++hf) sav[t][hf] = ldf4(rw, g * 16, MOD_OFF + w * 64 + (t < 2 ? t * 512 : u2) + hf * 1280);
      }
    }
    SB();
    STAMP(1, 1);
    u32x4 cre, cim;
    {
      const float step = (float)(k1 + 128 * ln) * (1.f / 2048.f);
      const u32 mlo = k1 == 64 ? 0u : 0xffffu, mhi = k1 == 0 ? 0u : 0xffff0000u;
#pragma unroll
      for (int i = 0; i < 4; ++i) {
        const float a = __builtin_amdgcn_fractf(step * (float)(4 * g + i));
        const float c = __builtin_amdgcn_cosf(a), s = __builtin_amdgcn_sinf(a);
        cre[i] = pk2(c, k1 == 64 ? c : s) & (mlo | mhi);
        cim[i] = pk2(-s, k1 == 64 ? -s : c) & (mlo | mhi);
      }
    }
    char* ct = sm + CT_OFF + l * 16;
#pragma unroll 1
    for (int set = w; set < 11; set += (w >= 4 ? 4 : 16)) {
      const float fln = (float)ln, fg4 = (float)(4 * g), fp = (float)p, fe = (float)(16 + (ln & 3));
      const bool ext = set == 3 || set == 4 || set == 10;
      const bool odd = set == 0 || set == 7 || set == 8;
      const float A = odd ? 2.f * fln + fp : ext ? fe : fln;
      const bool in16 = set == 2 || set == 4 || set == 8;
      const bool dbl = set == 5 || set >= 9;
      const float B = set == 6 ? fmaf(fg4, 128.f, (float)k1) : dbl ? 2.f * fg4 + fp : in16 ? 16.f : fg4;
      const float C = set == 6 ? 128.f : dbl ? 2.f : 1.f;
      const float Dn = set == 6 ? (1.f / 2048.f) : (set == 0 || set == 5) ? (1.f / 32.f) : set >= 7 ? (1.f / 640.f) : (1.f / 20.f);
      const float base = A * B * Dn, step = A * C * Dn;
      f32x4 cs, sn;
#pragma unroll
      for (int e = 0; e < 4; ++e) {
        const float a = __builtin_amdgcn_fractf(fmaf(step, (float)e, base));
        cs[e] = __builtin_amdgcn_cosf(a);
        sn[e] = __builtin_amdgcn_sinf(a);
      }
      if (set >= 7) {
        const float sc = set >= 9 ? (1.f / (2048.f * 640.f)) : 1.f;
        const int sl = SL_TFM + 2 * (set - 7);
        *(f32x4*)(ct + sl * 1024) = cs * sc;
        *(f32x4*)(ct + (sl + 1) * 1024) = sn * sc;
      } else {
        const u32 c01 = pk2(cs[0], cs[1]), c23 = pk2(cs[2], cs[3]), s01 = pk2(sn[0], sn[1]), s23 = pk2(sn[2], sn[3]);
        const u32x4 fre = {c01, c23, s01, s23}, fim = {s01 ^ 0x80008000u, s23 ^ 0x80008000u, c01, c23};
        const u32x4 z = {0u, 0u, 0u, 0u};
        const int sel = set == 2 ? g : (ln >> 2);
        const bool two = set == 2 || set == 3;
        const bool keep = two ? sel == 0 : (set != 4 || (sel == g && g < 2));
        const int sl = set <= 2 ? 2 * set : set == 3 ? SL_EM : 2 * set + 4;
        *(u32x4*)(ct + sl * 1024) = keep ? fre : z;
        *(u32x4*)(ct + (sl + 1) * 1024) = keep ? fim : z;
        if (two) {
          *(u32x4*)(ct + (sl + 2) * 1024) = sel == 1 ? fre : z;
          *(u32x4*)(ct + (sl + 3) * 1024) = sel == 1 ? fim : z;
        }
      }
    }
    STAMP(1, 2);
    const f32x4 z4 = {0.f, 0.f, 0.f, 0.f};
    {
      const float sg = p ? -1.f : 1.f;
      const f32x2 sg2 = {sg, sg};
#pragma unroll
      for (int t = 0; t < 3; ++t) {
        if (t == 2 && w >= 4) break;
        const f32x4 r1 = mfma16(ua[t][0], cre, z4), i1 = mfma16(ua[t][0], cim, z4);
        const f32x4 r2 = mfma16(ua[t][1], cre, z4), i2 = mfma16(ua[t][1], cim, z4);
        const f32x2 c2l = lo2(cav[t][1]) * sg2, c2h = hi2(cav[t][1]) * sg2;
        f32x2 vr0 = fma2(lo2(r2), c2l, lo2(r1) * lo2(cav[t][0])), vr1 = fma2(hi2(r2), c2h, hi2(r1) * hi2(cav[t][0]));
        const f32x2 vi0 = fma2(lo2(i2), c2l, lo2(i1) * lo2(cav[t][0])), vi1 = fma2(hi2(i2), c2h, hi2(i1) * hi2(cav[t][0]));
        if (k1 == 0) {
          const float on = ln == 0 ? 2048.f : 0.f;
          vr0 += (lo2(sav[t][0]) + sg2 * lo2(sav[t][1])) * on;
          vr1 += (hi2(sav[t][0]) + sg2 * hi2(sav[t][1])) * on;
        }
        char* dst = sm + ln * VP + (16 * w + 4 * g) * 2 + t * 256;
        *(u32x2*)(dst) = u32x2{pk2v(vr0), pk2v(vr1)};
        *(u32x2*)(dst + 640) = u32x2{pk2v(vi0), pk2v(vi1)};
      }
    }
    STAMP(1, 3);
    bar();
    STAMP(1, 4);
#define CT(sl) (*(const u32x4*)(ct + (sl) * 1024))
#define CTF(sl) (*(const f32x4*)(ct + (sl) * 1024))
    {
      const int q = ln >> 2, pp = l & 3;
      u32x4 y[3];
      {
        const u32x4 bre = CT(SL_D1), bim = CT(SL_D1 + 1);
        const f32x4 tcm = CTF(SL_TFM), tsm = CTF(SL_TFM + 1), tce = CTF(SL_TFE), tse = CTF(SL_TFE + 1);
        const char* srcm = sm + 2 * w * VP + ((4 * g + q) * 20 + 4 * pp) * 2;
        const char* srce = sm + (2 * w + (pp & 1)) * VP + ((4 * g + q) * 20 + 16) * 2;
#pragma unroll
        for (int t = 0; t < 3; ++t) {
          const u32x4 a = trread2(t < 2 ? srcm + t * VP : srce, 640);
          const f32x4 re = mfma16(a, bre, z4), im = mfma16(a, bim, z4);
          y[t] = t < 2 ? twpack(re, im, tcm, tsm) : twpack(re, im, tce, tse);
        }
      }
      const u32x4 mm0 = CT(SL_MM), mm1 = CT(SL_MM + 1), ee0 = CT(SL_EE), ee1 = CT(SL_EE + 1);
      u32x4 me[4], em[4];
#pragma unroll
      for (int i = 0; i < 4; ++i) { me[i] = CT(SL_ME + i); em[i] = CT(SL_EM + i); }
      u32x4 yf[3];
      {
        f32x4 re[3], im[3];
#pragma unroll
        for (int v = 0; v < 2; ++v) { re[v] = mfma16(mm0, y[v], z4); im[v] = mfma16(mm1, y[v], z4); }
        re[2] = mfma16(em[2], y[1], mfma16(em[0], y[0], z4));
        im[2] = mfma16(em[3], y[1], mfma16(em[1], y[0], z4));
#pragma unroll
        for (int v = 0; v < 2; ++v) { re[v] = mfma16(me[2 * v], y[2], re[v]); im[v] = mfma16(me[2 * v + 1], y[2], im[v]); }
        re[2] = mfma16(ee0, y[2], re[2]);
        im[2] = mfma16(ee1, y[2], im[2]);
#pragma unroll
        for (int t = 0; t < 3; ++t) {
          const f32x4 fr = {lo2f(fw[t][0]), lo2f(fw[t][1]), lo2f(fw[t][2]), lo2f(fw[t][3])};
          const f32x4 fi = {hi2f(fw[t][0]), hi2f(fw[t][1]), hi2f(fw[t][2]), hi2f(fw[t][3])};
          yf[t] = fltpack(re[t], im[t], fr, fi);
        }
      }
      {
        f32x4 re[3], im[3];
#pragma unroll
        for (int v = 0; v < 2; ++v) { re[v] = mfma16(yf[v], mm0, z4); im[v] = mfma16(yf[v], mm1, z4); }
        re[2] = mfma16(yf[1], em[2], mfma16(yf[0], em[0], z4));
        im[2] = mfma16(yf[1], em[3], mfma16(yf[0], em[1], z4));
#pragma unroll
        for (int v = 0; v < 2; ++v) { re[v] = mfma16(yf[2], me[2 * v], re[v]); im[v] = mfma16(yf[2], me[2 * v + 1], im[v]); }
        re[2] = mfma16(yf[2], ee0, re[2]);
        im[2] = mfma16(yf[2], ee1, im[2]);
        const u32x4 bre = CT(SL_D1I), bim = CT(SL_D1I + 1);
        const f32x4 tcm = CTF(SL_TIM), tsm = CTF(SL_TIM + 1), tce = CTF(SL_TIE), tse = CTF(SL_TIE + 1);
        char* dstm = sm + 2 * w * VP + (20 * ln + 4 * g) * 2;
        char* dste = sm + (2 * w + (g & 1)) * VP + (20 * ln + 16) * 2;
#pragma unroll
        for (int t = 0; t < 3; ++t) {
          const u32x4 a = t < 2 ? twpack(re[t], im[t], tcm, tsm) : twpack(re[t], im[t], tce, tse);
          const f32x4 vr = mfma16(a, bre, z4), vi = mfma16(a, bim, z4);
          char* dst = t < 2 ? dstm + t * VP : dste;
          if (t < 2 || g < 2) {
            *(u32x2*)(dst) = u32x2{pk2(vr[0], vr[1]), pk2(vr[2], vr[3])};
            *(u32x2*)(dst + 640) = u32x2{pk2(vi[0], vi[1]), pk2(vi[2], vi[3])};
          }
        }
      }
    }
    STAMP(1, 5);
    const u32x4 bre = CT(SL_CI), bim = CT(SL_CI + 1);
    bar();
    STAMP(1, 6);
    {
      const int q = ln >> 2, pp = l & 3;
      const char* src = sm + (4 * g + q) * VP + (16 * w + 4 * pp) * 2;
      const int vo = ln * 1280 + g * 16, so = UP_OFF + (k1 * 2 + p) * 16 * 1280 + w * 64;
#pragma unroll
      for (int t = 0; t < 3; ++t) {
        if (t == 2 && w >= 4) break;
        const u32x4 a = trread2(src + t * 256, 640);
        const f32x4 re = mfma16(a, bre, z4), im = mfma16(a, bim, z4);
        st128(u32x4{pk2(re[0], im[0]), pk2(re[1], im[1]), pk2(re[2], im[2]), pk2(re[3], im[3])}, rw, vo, so + t * 512);
      }
    }
    if (blockIdx.x == 0) ((u32*)(ws + MARK_OFF))[tid] = SENT;
#undef CT
#undef CTF
  } else if (idx >= 18 && idx < 30) {
    const int id = xcd * 12 + (idx - 18);
    const float* modf = (const float*)(ws + MOD_OFF);
    float* red = (float*)sm;
    const int j = id / 6;
    if (tid < 320) {
      const int ng = tid & 3, kg = tid >> 2, mat = ng & 1, hf = ng >> 1;
      const int kstep = kg >> 2, g = kg & 3;
      const u16* W = (mat ? wu : wg) + 16 * id + 8 * hf + (size_t)(8 * kg) * FF_;
      u32x4 v[8];
#pragma unroll
      for (int e = 0; e < 8; ++e) v[e] = *(const u32x4*)(W + (size_t)e * FF_);
      const f32x4 f0 = *(const f32x4*)(modf + 2560 + 8 * kg), f1 = *(const f32x4*)(modf + 2560 + 8 * kg + 4);
      const f32x4 b0 = *(const f32x4*)(modf + 1920 + 8 * kg), b1 = *(const f32x4*)(modf + 1920 + 8 * kg + 4);
      const int ffl = (16 * id + 8 * hf) % 96, wv = ffl / 24, tile = (ffl % 24) >> 3;
      const int dst = (((((j * 4 + wv) * 20 + kstep) * 3 + tile) * 64 + 16 * g + 2 * mat)) * 16;
#pragma unroll
      for (int c = 0; c < 8; ++c) {
        f32x2 wp[4];
#pragma unroll
        for (int e = 0; e < 4; ++e)
          wp[e] = (c & 1) ? f32x2{hi2f(v[2 * e][c >> 1]), hi2f(v[2 * e + 1][c >> 1])} : f32x2{lo2f(v[2 * e][c >> 1]), lo2f(v[2 * e + 1][c >> 1])};
        const u32x4 o = {pk2v(wp[0] * lo2(f0)), pk2v(wp[1] * hi2(f0)), pk2v(wp[2] * lo2(f1)), pk2v(wp[3] * hi2(f1))};
        float cs = 0.f;
#pragma unroll
        for (int e = 0; e < 4; ++e) cs = dot2(o[e], 0x3f803f80u, cs);
        const f32x2 bs = fma2(wp[0], lo2(b0), fma2(wp[1], hi2(b0), fma2(wp[2], lo2(b1), wp[3] * hi2(b1))));
        st128(o, rw, dst + (4 * (c >> 1) + (c & 1)) * 16, WP_OFF);
        red[kg * 64 + ng * 8 + c] = cs;
        red[kg * 64 + 32 + ng * 8 + c] = bs[0] + bs[1];
      }
    }
    __syncthreads();
    {
      const int o = tid & 63, part = tid >> 6;
      float s = 0.f;
#pragma unroll
      for (int i = 0; i < 10; ++i) s += red[(part * 10 + i) * 64 + o];
      red[5120 + part * 64 + o] = s;
    }
    __syncthreads();
    if (tid < 64) {
      float s = 0.f;
#pragma unroll
      for (int i = 0; i < 8; ++i) s += red[5120 + i * 64 + tid];
      const int which = tid >> 5, ng = (tid >> 3) & 3, c = tid & 7, mat = ng & 1, hf = ng >> 1;
      const int ffl = (16 * id + 8 * hf) % 96, wv = ffl / 24, tile = (ffl % 24) >> 3;
      const float h0 = rbf(s), h1 = rbf(s - h0), h2 = (s - h0) - h1;
      st64(u32x2{pk2(h0, h1), pk2(h2, 0.f)}, rw, (((j * 4 + wv) * 3 + tile) * 16 + 4 * (c >> 1) + 2 * mat + (c & 1)) * 16 + which * 8, CS_OFF);
    }
  } else {
    const int id = idx >= 30 ? (idx - 30) * 8 + xcd : 16 + (idx - 16) * 7 + (xcd - 1);
    const int it = id * 512 + tid, fg = it / 80, cgp = it - fg * 80;
    u32x4 v[8];
#pragma unroll
    for (int e = 0; e < 8; ++e) v[e] = *(const u32x4*)(wd + (size_t)(8 * fg + e) * D_ + 8 * cgp);
    const int j = fg / 12, rem = fg % 12, gq = rem / 3, t = rem % 3;
    const int col = 8 * cgp, cgrp = col / 80, tile = (col % 80) >> 4;
    const int dst = (((cgrp * 48 + 3 * j + t) * 5 + tile) * 64 + 16 * gq + ((col % 80) & 15)) * 16;
#pragma unroll
    for (int c = 0; c < 8; ++c) {
      u32x4 o;
#pragma unroll
      for (int e = 0; e < 4; ++e)
        o[e] = __builtin_amdgcn_perm(v[2 * e + 1][c >> 1], v[2 * e][c >> 1], (c & 1) ? 0x07060302u : 0x05040100u);
      st128(o, rw, dst + c * 16, WD_OFF);
    }
  }
  STAMP(1, 7);
}

constexpr int L_TOK = 0, L_XB = 81920, L_ST = 131072, L_MS = 133120, L_END = 135168;
constexpr u32 SPIN_CAP = 1u << 20;
#define H2E 3
#define NWE 8
template <int V> struct IC { static constexpr int value = V; };
template <int I, int N, class F> DI void static_for(F& f) { if constexpr (I < N) { f(IC<I>{}); static_for<I + 1, N>(f); } }
DI u32 umax(u32 a, u32 b) { return a > b ? a : b; }
DI u32 umin(u32 a, u32 b) { return a < b ? a : b; }
DI u32 mx4(u32 m, const u32x4& v) { return umax(umax(umax(m, v[0]), v[1]), umax(v[2], v[3])); }
DI bool any_sent(u32 m) { return __builtin_amdgcn_ballot_w64(m == SENT) != 0ull; }
DI void dma1k(rsrc_t r, int voff, int soff, u32 lds) {
  u32 keep;
  asm volatile("s_mov_b32 %0, m0\n\ts_mov_b32 m0, %4\n\ts_nop 0\n\tbuffer_load_dwordx4 %1, %2, %3 offen sc1 lds\n\ts_mov_b32 m0, %0"
               : "=&s"(keep) : "v"(voff), "s"(r), "s"(soff), "s"(lds) : "memory");
}
#define VMWAIT(n) asm volatile("s_waitcnt vmcnt(%0)" :: "n"(n) : "memory")

extern "C"
#if defined(__HIP_DEVICE_COMPILE__)
__attribute__((target("no-packed-fp32-ops")))
#endif
__global__ __launch_bounds__(512, 2) void k3_tail(const u16* __restrict__ x, char* __restrict__ ws, u16* __restrict__ out) {
  __shared__ __attribute__((aligned(16))) char sm[L_END];
  const int tid = threadIdx.x, xcd = blockIdx.x & 7, idx = blockIdx.x >> 3;
  const int s2 = 8 * (xcd >> 2) + (idx & 7), j = 4 * (xcd & 3) + (idx >> 3);
  const int w = __builtin_amdgcn_readfirstlane(tid >> 6), l = tid & 63, ln = l & 15, g = l >> 4;
  const int wq = w & 3, kh = w >> 2, vo = l * 16;
  const rsrc_t rw = mkrs(ws, WS_USED);
  const f32x4 z4 = {0.f, 0.f, 0.f, 0.f};
  STAMP(2, 0);
  u32x4 wreg[30], wx[3];
  const int wb = WP_OFF + ((j * 4 + wq) * 60 + kh * 30) * 1024;
#define W_LOAD(A, B) _Pragma("unroll") for (int i = (A); i < (B); ++i) wreg[i] = ld128(rw, vo, wb + i * 1024)
  const int cg = (j + 2 * (idx & 7)) & 15;
  if (cg < 10) {
    u32x2 fa[4], ny;
    {
      const int so = UP_OFF + ((w & 1) * 16 + s2) * 1280 + cg * 128;
#pragma unroll
      for (int i = 0; i < 4; ++i) fa[i] = ld64(rw, 4 * g * 2 * 16 * 1280 + ln * 8, so + (16 * (w >> 1) + i) * 2 * 16 * 1280);
      ny = ld64(rw, ln * 8, so + 128 * 16 * 1280);
    }
    SB();
    const rsrc_t rx = mkrs(x, 2048 * 640 * 2);
    u32x4 xv[2];
    f32x4 gav[2][2];
#pragma unroll
    for (int hf = 0; hf < 2; ++hf) xv[hf] = ld128(rx, ln * 16 * 1280 + g * 16, (16 * 16 * w + s2) * 1280 + cg * 64 + hf * 640);
#pragma unroll
    for (int hf = 0; hf < 2; ++hf)
#pragma unroll
      for (int m = 0; m < 2; ++m) gav[hf][m] = ldf4(rw, g * 32, MOD_OFF + (1280 + 32 * cg + 320 * hf + 4 * m) * 4);
    SB();
    u32x4 bc[4];
    {
      const int s1 = 16 * w + ln;
      const float step = (float)s1 * (1.f / 128.f), base = step * (float)(4 * g);
      const float t16 = 2.f * __builtin_amdgcn_cosf(step * 16.f);
#pragma unroll
      for (int i = 0; i < 4; ++i) {
        const float a0 = fmaf(step, (float)i, base), a1 = fmaf(step, (float)(16 + i), base);
        const float c0 = __builtin_amdgcn_cosf(a0), s0 = __builtin_amdgcn_sinf(a0), c1 = __builtin_amdgcn_cosf(a1), s1_ = __builtin_amdgcn_sinf(a1);
        const float c2 = fmaf(t16, c1, -c0), s2_ = fmaf(t16, s1_, -s0);
        bc[0][i] = pk2(c0, s0); bc[1][i] = pk2(c1, s1_); bc[2][i] = pk2(c2, s2_); bc[3][i] = pk2(fmaf(t16, c2, -c1), fmaf(t16, s2_, -s1_));
      }
      if (g == 0) bc[0][0] = (s1 & 1) ? 0xbf003f00u : 0x3f003f00u;
      pin(bc[0]); pin(bc[1]); pin(bc[2]); pin(bc[3]);
    }
    SB();
    W_LOAD(0, 3);
    SB();
#pragma unroll
    for (int mu = 0; mu < 2; ++mu) {
      if (w < 2 && g == 0) fa[0][mu] = __builtin_amdgcn_perm(ny[mu], fa[0][mu], 0x05040100u);
      *(u32x4*)(sm + (w * 2 + mu) * 1024 + vo) = u32x4{fa[0][mu], fa[1][mu], fa[2][mu], fa[3][mu]};
    }
    STAMP(2, 10);
    bar();
    STAMP(2, 11);
    W_LOAD(3, 6);
    SB();
    f32x4 acc[2][2];
#pragma unroll
    for (int kk = 0; kk < 4; ++kk)
#pragma unroll
      for (int pp = 0; pp < 2; ++pp)
#pragma unroll
        for (int mu = 0; mu < 2; ++mu)
          acc[pp][mu] = mfma16(*(const u32x4*)(sm + ((kk * 2 + pp) * 2 + mu) * 1024 + vo), bc[kk], kk == 0 ? z4 : acc[pp][mu]);
    float ss = 0.f, qq = 0.f;
#pragma unroll
    for (int hf = 0; hf < 2; ++hf) {
      u32x4 o;
#pragma unroll
      for (int r = 0; r < 4; ++r) {
        const float y0 = hf ? acc[0][0][r] - acc[1][0][r] : acc[0][0][r] + acc[1][0][r];
        const float y1 = hf ? acc[0][1][r] - acc[1][1][r] : acc[0][1][r] + acc[1][1][r];
        o[r] = pk2(fmaf(gav[hf][r >> 1][2 * (r & 1)], y0, lo2f(xv[hf][r])), fmaf(gav[hf][r >> 1][2 * (r & 1) + 1], y1, hi2f(xv[hf][r])));
      }
      st128c(o, rw, vo, X1_OFF + ((s2 * 20 + cg + 10 * hf) * 8 + w) * 1024);
#pragma unroll
      for (int e = 0; e < 4; ++e) { ss = dot2(o[e], 0x3f803f80u, ss); qq = dot2(o[e], o[e], qq); }
    }
    {
      const auto a = __builtin_amdgcn_permlane32_swap(f2u(ss), f2u(qq), false, false);
      const u32 z = f2u(u2f(a[0]) + u2f(a[1]));
      const auto b = __builtin_amdgcn_permlane16_swap(z, z, false, false);
      __builtin_amdgcn_raw_buffer_store_b32(f2u(u2f(b[0]) + u2f(b[1])), rw, (g & 1) ? 0x7ff00000 : ln * 8 + (g >> 1) * 4,
                                            STAT_OFF + ((s2 * 10 + cg) * 128 + 16 * w) * 8, 16);
    }
    STAMP(2, 12);
    SB();
    W_LOAD(6, 10);
    VMWAIT(4);
    bar();
    if (tid == 0) __builtin_amdgcn_raw_buffer_store_b32(1u, rw, 0, MARK_OFF + s2 * 128 + cg * 4, 16);
  } else {
    W_LOAD(0, 10);
  }
  STAMP(2, 1);
  if (w == 7) {
#pragma unroll 1
    for (u32 sp = 0; sp < SPIN_CAP; ++sp) {
      asm volatile("" ::: "memory");
      if (!any_sent(__builtin_amdgcn_raw_buffer_load_b32(rw, ln < 10 ? ln * 4 : 0, MARK_OFF + s2 * 128, 16))) break;
      __builtin_amdgcn_s_sleep(1);
    }
  }
#pragma unroll
  for (int t = 0; t < 3; ++t) wx[t] = ld128(rw, g == 0 ? vo : 0x7ff00000, CS_OFF + ((j * 4 + wq) * 3 + t) * 256);
  W_LOAD(10, 30);
  bar();
  STAMP(2, 6);
  const int sub = (w >> 1) & 1;
  const int xsrc = X1_OFF + s2 * 160 * 1024 + (10 * kh + 5 * (w & 1)) * 8192;
  const int tS = (kh ^ 1) * 1024, tF = kh * 1024;
  const u32 ldst = (u32)(size_t)(LAS char*)sm + L_TOK + 5 * w * 1024;
#define TOK_TILE(b) ((b) == 0 ? tS + sub * 2048 : (b) == 3 ? tF + 4096 + sub * 2048 : sub ? tS + ((b) + 1) * 2048 : tF + ((b) - 1) * 2048)
#define TOK_DMA(b, i) dma1k(rw, vo, xsrc + (i) * 8192 + TOK_TILE(b), ldst + ((b) & 1) * 40960 + (i) * 1024)
  const int kq0 = wq < 3 ? 3 * wq : 7;
  const int x0src = X1_OFF + s2 * 160 * 1024 + (10 * kh + kq0) * 8192 + tS;
  const u32 l0dst = (u32)(size_t)(LAS char*)sm + L_TOK + (kh * 20 + kq0) * 1024;
#pragma unroll
  for (int i = 0; i < 3; ++i) dma1k(rw, vo, x0src + i * 8192, l0dst + i * 1024);
  f32x4 pa, pb;
  asm volatile("buffer_load_dwordx4 %0, %1, %2, %3 offen sc1" : "=v"(pa) : "v"((l & 7) * 16 + (l >> 3) * 1024), "s"(rw), "s"(STAT_OFF + s2 * 10240 + w * 128) : "memory");
  asm volatile("buffer_load_dwordx4 %0, %1, %2, %3 offen sc1" : "=v"(pb) : "v"(l < 16 ? (l & 7) * 16 + (l >> 3) * 1024 : 0x7ff00000), "s"(rw), "s"(STAT_OFF + s2 * 10240 + 8192 + w * 128) : "memory");
  SB();
#pragma unroll
  for (int i = 0; i < 3; ++i) dma1k(rw, vo, x0src + i * 8192 + 2048, l0dst + 10240 + i * 1024);
  SB();
  f32x4 A[8][3], cst, acc[2][5];
  u32x4 hq, bq[3], bm, wde[NWE + 3], h[4][6], xr[4];
  f32x4 gbv;
  float z0, z1, p0, p1;
  u32 mk;
  const char* tok = sm + L_TOK + kh * 20480 + vo;
  const int hbase = g < 3 ? HH_OFF + (s2 * 48 + 3 * j) * 8192 + kh * 1024 + (16 * wq + ln) * 16 + g * 8192 : 0x7ff00000;
#define PIN1(x) asm volatile("" : "+v"(x))
#define EPI_A(K) do { PIN1(K); z0 = K[0] * fmaf(K[0] * K[0], cst[1], cst[0]); z1 = K[1] * fmaf(K[1] * K[1], cst[1], cst[0]); PIN1(z0); PIN1(z1); } while (0)
#define EPI_B(K) do { PIN1(z0); p0 = K[0] * K[2]; p1 = K[1] * K[3]; \
    z0 = __builtin_amdgcn_exp2f(z0); z1 = __builtin_amdgcn_exp2f(z1); PIN1(z0); PIN1(z1); PIN1(p0); PIN1(p1); } while (0)
#define EPI_C() do { PIN1(z0); z0 = __builtin_amdgcn_rcpf(fmaf(z0, cst[2], cst[2])); z1 = __builtin_amdgcn_rcpf(fmaf(z1, cst[2], cst[2])); PIN1(z0); PIN1(z1); } while (0)
#define EPI_D(t) do { PIN1(z0); u32 h_ = umin(pk2(p0 * z0, p1 * z1), SENT - 1u); PIN1(h_); hq[t] = h_; } while (0)
#define EPI_STORE(P) do {   \
    const auto a_ = __builtin_amdgcn_permlane32_swap(hq[0], hq[2], false, false); \
    const auto b_ = __builtin_amdgcn_permlane32_swap(hq[1], hq[1], false, false); \
    const auto c_ = __builtin_amdgcn_permlane16_swap(a_[0], b_[0], false, false); \
    const auto d_ = __builtin_amdgcn_permlane16_swap(a_[1], b_[1], false, false); \
    st128c(u32x4{c_[0], c_[1], d_[0], d_[1]}, rw, hbase, (P) * 2048); } while (0)
#define EPI_CST(P) cst = *(const f32x4*)(sm + L_ST + (32 * (P) + 16 * kh + ln) * 16)
#define EPI_STEP(e, PH, P) do { if ((e) >= 0 && (e) < 12) { if (((e) & 3) == 0) EPI_A(A[PH][((e) >> 2) % 3]); if (((e) & 3) == 1) EPI_B(A[PH][((e) >> 2) % 3]); \
      if (((e) & 3) == 2) EPI_C(); if (((e) & 3) == 3) EPI_D(((e) >> 2) % 3); } if ((e) == 12) EPI_STORE(P); } while (0)
#define XB_WRITE(PH, T, t) *(f32x4*)(sm + L_XB + ((T) & 1) * 24576 + (w * 3 + (t)) * 1024 + vo) = A[PH][t]
#define XB_READ(PH, T) _Pragma("unroll") for (int t = 0; t < 3; ++t) A[PH][t] = *(const f32x4*)(sm + L_XB + ((T) & 1) * 24576 + ((w ^ 4) * 3 + t) * 1024 + vo)
#define SGB(mask, n) __builtin_amdgcn_sched_group_barrier(mask, n, 0)
#define FRAG(n) (*(const u32x4*)(tok + (((n) / 20) & 1) * 40960 + (((n) / 10) & 1) * 10240 + ((n) % 10) * 1024))
  const int cgrp = 4 * ((xcd >> 1) & 1) + (idx >> 3), hf2 = xcd & 1;
  const int db = WD_OFF + (cgrp * 48 + 6 * w) * 5 * 1024;
  const int hb = HH_OFF + ((s2 * 48 + 6 * w) * 8 + hf2) * 1024;
#define HH_LD(T, i) ld128c(rw, vo, hb + (i) * 8192 + (T) * 2048)
  asm volatile("s_waitcnt vmcnt(3)" : "+v"(pa), "+v"(pb) :: "memory");
  bar();
  STAMP(2, 3);
  bq[0] = FRAG(0);
  bq[1] = FRAG(1);
  auto g1_step = [&](auto nc) __attribute__((always_inline)) {
    constexpr int n = decltype(nc)::value;
    constexpr int ph = n / 10, kk = n % 10;
    constexpr bool isS = ph == 0 || ph == 1 || ph == 3 || ph == 5;
    if (n == 59 - 2 && w == 7) {
#pragma unroll 1
      for (u32 sp = 0; any_sent(mk) && sp < SPIN_CAP; ++sp) {
        __builtin_amdgcn_s_sleep(1);
        asm volatile("" ::: "memory");
        mk = __builtin_amdgcn_raw_buffer_load_b32(rw, ln * 4, MARK_OFF + s2 * 128 + 64, 16);
      }
    }
    if constexpr (n == 1) { pa += pb; PIN1(pa); }
    if constexpr (n >= 2 && n < 6) {
      PIN1(pa);
      float v = pa[n - 2];
      v += dppf<0x128>(v);
      const auto a = __builtin_amdgcn_permlane16_swap(f2u(v), f2u(v), false, false);
      v = u2f(a[0]) + u2f(a[1]);
      const auto b = __builtin_amdgcn_permlane32_swap(f2u(v), f2u(v), false, false);
      pa[n - 2] = u2f(b[0]) + u2f(b[1]);
      PIN1(pa);
    }
    if constexpr (n == 8) { PIN1(pa); ln_scalars((l & 8) ? pa[2] : pa[0], (l & 8) ? pa[3] : pa[1], z0, z1); PIN1(z0); PIN1(z1); }
    if constexpr (n == 11) {
      PIN1(z1);
      const float C0 = -2.f * 0.7978845608028654f * 1.4426950408889634f, C1 = C0 * 0.044715f;
      const float r = __builtin_amdgcn_rcpf(z1);
      const u32 nm = pk2(-z0, -z0), sp = pk2(z1, z1);
      const int tk = 16 * w + 2 * (l & 7) + ((l >> 3) & 1);
      const u32x4 cv = {f2u(C0 * r), f2u(C1 * r * r * r), f2u(z1 * z1), 0u}, mv = {nm, nm & 0xffffu, sp, sp & 0xffffu};
      *(u32x4*)(sm + ((g & 1) ? L_MS : L_ST) + tk * 16) = (g & 1) ? mv : cv;
    }
    if (n == 7) VMWAIT(3);
    if (n == 18) VMWAIT(3);
    if (n == 38) VMWAIT(NWE / 2);
    if (n == 58) VMWAIT(NWE / 2 + 2);
    if (n == 7 || n == 18 || n == 38 || n == 58 || n == 68) bar();
    if (n == 38 && tid == 0) __builtin_amdgcn_raw_buffer_store_b32(1u, rw, 0, MARK_OFF + s2 * 128 + 64 + j * 4, 16);
    if (n == 20) STAMP(2, 14);
    if (n == 60) STAMP(2, 13);
    if constexpr (n + 2 < 80) bq[(n + 2) % 3] = FRAG(n + 2);
    if (!isS && kk == 7) bm = *(const u32x4*)(sm + L_MS + (32 * (ph == 7 ? 3 : (ph >> 1) - 1) + 16 * kh + ln) * 16);
    if (n == 18) { XB_READ(2, 0); }
    if (n == 38) { XB_READ(4, 1); }
    if (n == 58) { XB_READ(6, 2); }
    if (n == 68) { XB_READ(7, 3); }
#pragma unroll
    for (int t = 0; t < 3; ++t) A[ph][t] = mfma16(wreg[kk * 3 + t], bq[n % 3], (kk == 0 && isS) ? z4 : A[ph][t]);
    if (!isS && kk == 9) {
#pragma unroll
      for (int t = 0; t < 3; ++t) A[ph][t] = mfma16(wx[t], bm, A[ph][t]);
    }
    if constexpr (n == 12 || n == 14 || n == 16) XB_WRITE(0, 0, (n - 12) / 2);
    if constexpr (n == 22 || n == 24 || n == 26) XB_WRITE(1, 1, (n - 22) / 2);
    if constexpr (n == 42 || n == 44 || n == 46) XB_WRITE(3, 2, (n - 42) / 2);
    if constexpr (n == 61 || n == 63 || n == 65) XB_WRITE(5, 3, (n - 61) / 2);
    if constexpr (n >= 1 && n < 10 && (n & 1)) TOK_DMA(1, (n - 1) / 2);
    if constexpr (n == 11 || n == 13 || n == 15) st128c(u32x4{SENT, SENT, SENT, SENT}, rw, vo, HH_OFF + (s2 * 48 + 3 * j + (n - 11) / 2) * 8192 + w * 1024);
    if constexpr (n >= 19 && n < 28 && (n & 1)) TOK_DMA(2, (n - 19) / 2);
    if constexpr (n >= 29 && n < 29 + NWE / 2) wde[n - 29] = ld128(rw, vo, db + (n - 29) * 1024);
    if constexpr (n >= 39 && n < 48 && (n & 1)) TOK_DMA(3, (n - 39) / 2);
    if constexpr (n >= 51 && n < 51 + NWE / 2) wde[NWE / 2 + n - 51] = ld128(rw, vo, db + (NWE / 2 + n - 51) * 1024);
    if (n == 30) EPI_CST(0);
    if (n == 50) EPI_CST(1);
    if (n == 69) EPI_CST(2);
    if constexpr (n >= 32 && n < 44) EPI_STEP(n - 32, 2, 0);
    if constexpr (n == 48) EPI_STORE(0);
    if constexpr (n >= 52 && n < 65) EPI_STEP(n - 52, 4, 1);
    if constexpr (n >= 71 && n < 77) { EPI_STEP(2 * (n - 71), 6, 2); EPI_STEP(2 * (n - 71) + 1, 6, 2); }
    if constexpr (n == 77) EPI_STORE(2);
    if (n == 50) mk = __builtin_amdgcn_raw_buffer_load_b32(rw, ln * 4, MARK_OFF + s2 * 128 + 64, 16);
    if constexpr (n == 60 || n == 62 || n == 66) wde[NWE + (n == 66 ? 2 : (n - 60) / 2)] = ld128(rw, vo, db + (NWE + (n == 66 ? 2 : (n - 60) / 2)) * 1024);
    if constexpr (n >= 67 && n < 71) h[0][n - 67] = HH_LD(0, n - 67);
    if constexpr (n == 72 || n == 74) h[0][4 + (n - 72) / 2] = HH_LD(0, 4 + (n - 72) / 2);
    if constexpr (n == 71 || n == 73 || n == 75 || n == 76 || n == 78 || n == 79) {
      constexpr int i = n < 76 ? (n - 71) / 2 : n == 76 ? 3 : n - 74;
      wreg[i] = ld128(rw, vo, db + (NWE + 3 + i) * 1024);
    }
#pragma unroll
    for (int i = 0; i < 3; ++i) { SGB(0x8, 1); SGB(0x100, 1); SGB(0x402, n >= 70 ? 4 : 2); SGB(0x220, 1); }
    SB();
  };
  static_for<0, 80>(g1_step);
  STAMP(2, 4);
  const int rsel = w < 4 ? w : 4;
  const int rc0 = 80 * cgrp + 16 * rsel + 4 * g;
  const rsrc_t ro = mkrs(out, 2048 * 640 * 2);
#define WD(i) ((i) < NWE + 3 ? wde[(i) < NWE + 3 ? (i) : 0] : wreg[(i) < NWE + 3 ? 0 : (i) - NWE - 3])
#define G2_REDUCE(T) do { if (w < 4 || w == 4 + (T)) { \
    f32x4 s_ = *(const f32x4*)(sm + ((T) & 1) * 40960 + rsel * 1024 + vo); \
    _Pragma("unroll") for (int sw = 1; sw < 8; ++sw) s_ += *(const f32x4*)(sm + ((T) & 1) * 40960 + (sw * 5 + rsel) * 1024 + vo); \
    const u32 x0_ = (g & 1) ? xr[T][2] : xr[T][0], x1_ = (g & 1) ? xr[T][3] : xr[T][1]; \
    st64(u32x2{pk2(fmaf(gbv[0], s_[0], lo2f(x0_)), fmaf(gbv[1], s_[1], hi2f(x0_))), pk2(fmaf(gbv[2], s_[2], lo2f(x1_)), fmaf(gbv[3], s_[3], hi2f(x1_)))}, \
         ro, ln * 16 * 1280 + rc0 * 2, (16 * 16 * (hf2 + 2 * (T)) + s2) * 1280); } } while (0)
#define HH_CHECK(H, T) do { u32 m_ = 0u; _Pragma("unroll") for (int i = 0; i < 6; ++i) m_ = mx4(m_, H[i]); \
    if (__builtin_expect(any_sent(m_), 0)) { u32 sp_ = 0; _Pragma("unroll 1") do { __builtin_amdgcn_s_sleep(2); asm volatile("" ::: "memory"); \
      _Pragma("unroll") for (int i = 0; i < 6; ++i) H[i] = HH_LD(T, i); \
      m_ = 0u; _Pragma("unroll") for (int i = 0; i < 6; ++i) m_ = mx4(m_, H[i]); } while (any_sent(m_) && ++sp_ < SPIN_CAP); } } while (0)
#define G2_PART(T, t) *(f32x4*)(sm + ((T) & 1) * 40960 + (w * 5 + (t)) * 1024 + vo) = acc[(T) & 1][t]
  EPI_CST(3);
  constexpr int WR0 = 6, WRN = 27 - NWE;
  auto g2_step = [&](auto nc) __attribute__((always_inline)) {
    constexpr int n = decltype(nc)::value;
    constexpr int T = n / 6, kk = n % 6;
    if (kk == 0) HH_CHECK(h[T], T);
    if (T > 0 && kk == 4) bar();
#pragma unroll
    for (int t = 0; t < 5; ++t) acc[T & 1][t] = mfma16(WD(kk * 5 + t), h[T][kk], kk == 0 ? z4 : acc[T & 1][t]);
    if constexpr (n < 3) {
      EPI_STEP(4 * n, 7, 3); EPI_STEP(4 * n + 1, 7, 3); EPI_STEP(4 * n + 2, 7, 3); EPI_STEP(4 * n + 3, 7, 3);
#pragma unroll
      for (int i = WR0 + 2 * n; i < WR0 + 2 * n + 2; ++i) wreg[i] = ld128(rw, vo, db + (NWE + 3 + i) * 1024);
      if (n == 2) {
        EPI_STORE(3);
#pragma unroll
        for (int i = WR0 + 6; i < WRN; ++i) wreg[i] = ld128(rw, vo, db + (NWE + 3 + i) * 1024);
      }
    }
    if constexpr (n >= 3 && n < 6) { h[1][2 * (n - 3)] = HH_LD(1, 2 * (n - 3)); h[1][2 * (n - 3) + 1] = HH_LD(1, 2 * (n - 3) + 1); }
    if constexpr (n == 5) {
      gbv = ldf4(rw, rc0 * 4, MOD_OFF + 3200 * 4);
#pragma unroll
      for (int q = 0; q < 4; ++q) xr[q] = ld128c(rw, (rc0 >> 5) * 8192 + (((rc0 >> 3) & 3) * 16 + ln) * 16, X1_OFF + (s2 * 160 + hf2 + 2 * q) * 1024);
#pragma unroll
      for (int i = 0; i < H2E; ++i) h[2][i] = HH_LD(2, i);
    }
    if constexpr (n >= 6 && n < 12 - H2E) h[2][n - 6 + H2E] = HH_LD(2, n - 6 + H2E);
    if constexpr (n >= 12 - H2E && n < 18 - H2E) h[3][n - 12 + H2E] = HH_LD(3, n - 12 + H2E);
    if constexpr (T > 0 && kk < 3) {
      G2_PART(T - 1, 2 * kk);
      if constexpr (kk < 2) G2_PART(T - 1, 2 * kk + 1);
    }
    if constexpr (T > 0 && kk == 4) G2_REDUCE(T - 1);
#pragma unroll
    for (int i = 0; i < 5; ++i) { SGB(0x8, 1); SGB(0x402, n < 3 ? 5 : 2); SGB(0x320, 1); }
    if (n == 5) STAMP(2, 7);
    if (n == 11) STAMP(2, 15);
    if (n == 17) STAMP(2, 5);
    SB();
  };
  static_for<0, 24>(g2_step);
  STAMP(2, 8);
#pragma unroll
  for (int t = 0; t < 5; ++t) G2_PART(3, t);
  bar();
  G2_REDUCE(3);
  STAMP(2, 9);
}

extern "C" __global__ void k9_end() {}

extern "C" void kernel_launch(void* const* d_in, const int* in_sizes, int n_in, void* d_out, int out_size, void* d_ws,
                              size_t ws_size, hipStream_t stream) {
  const u16* x = (const u16*)d_in[0];
  const u16* wr = (const u16*)d_in[1];
  const u16* wi = (const u16*)d_in[2];
  const u16* wg = (const u16*)d_in[4];
  const u16* wu = (const u16*)d_in[5];
  const u16* wd = (const u16*)d_in[6];
  const u16* c = (const u16*)d_in[7];
  const u16* wmod = (const u16*)d_in[8];
  char* ws = (char*)d_ws;
  k1_ln_dft128<<<256, 512, 0, stream>>>(x, c, wmod, wr, wi, ws);
  k2_spec<<<256, 512, 0, stream>>>(wg, wu, wd, ws);
  k3_tail<<<256, 512, 0, stream>>>(x, ws, (u16*)d_out);
}
```
